```python
import torch

def _softmax(x, axis=-1):
    return torch.softmax(x, dim=axis)
def _gelu(x):
    return torch.nn.functional.gelu(x, approximate="tanh")
def _silu(x):
    return torch.nn.functional.silu(x)
def _sigmoid(x):
    return torch.sigmoid(x)
def _layernorm(x, axis=-1, eps=1e-5):
    m = x.mean(dim=axis, keepdim=True)
    v = x.var(dim=axis, keepdim=True, unbiased=False)
    return (x - m) / torch.sqrt(v + eps)
def _scatter_add(tgt, idx, src, axis=0):
    return tgt.clone().index_add_(axis, idx, src)
def _segment_sum(src, seg, n):
    out = torch.zeros((n,) + src.shape[1:], dtype=src.dtype)
    return out.index_add_(0, seg, src)
def _pad(x, pw):
    flat = tuple(v for pair in reversed(pw) for v in pair)
    return torch.nn.functional.pad(x, flat)

kernel_name = 'sequential_preLN__sliding_block__geglu_ffn__prompt'

def setup_inputs():

    _w = (lambda a: (lambda t: t.to(torch.bfloat16) if t.is_floating_point() else t)(torch.as_tensor(a)))
    def _A_setup():
        B, S, D = 1, 2048, 640
        return {
            "x": _w((lambda *s: torch.randn(*s, dtype=torch.bfloat16) * 0.05)(B, S, D)),
            "wqkv": _w((lambda *s: torch.randn(*s, dtype=torch.bfloat16) * 0.05)(D, 3 * D)),
            "wo": _w((lambda *s: torch.randn(*s, dtype=torch.bfloat16) * 0.05)(D, D)),
        }
    _A_in = _A_setup()

    def _B_setup():
        B, S, D, F = 1, 2048, 640, 1728
        return {
            "x": _w((lambda *s: torch.randn(*s, dtype=torch.bfloat16) * 0.05)(B, S, D)),
            "w_gate": _w((lambda *s: torch.randn(*s, dtype=torch.bfloat16) * 0.05)(D, F)),
            "w_up": _w((lambda *s: torch.randn(*s, dtype=torch.bfloat16) * 0.05)(D, F)),
            "w_down": _w((lambda *s: torch.randn(*s, dtype=torch.bfloat16) * 0.05)(F, D)),
        }
    _B_in = _B_setup()

    return {
        "A_x": _A_in["x"],
        "A_wqkv": _A_in["wqkv"],
        "A_wo": _A_in["wo"],
        "B_x": _B_in["x"],
        "B_w_gate": _B_in["w_gate"],
        "B_w_up": _B_in["w_up"],
        "B_w_down": _B_in["w_down"],
    }

def reference(A_x, A_wqkv, A_wo, B_x, B_w_gate, B_w_up, B_w_down):
    def _A_ref(x, wqkv, wo):
        B, S, D = x.shape
        H = 32; Dh = D // H; W = 256
        qkv = (lambda x, *d: x.permute(*d))((x @ wqkv).reshape(B, S, 3, H, Dh), 2, 0, 3, 1, 4)
        q, k, v = qkv[0], qkv[1], qkv[2]
        scores = torch.einsum("bhqd,bhkd->bhqk", q, k) * (Dh ** -0.5)
        idx = torch.arange(S)
        mask = (idx[:, None] - idx[None, :] >= 0) & (idx[:, None] - idx[None, :] < W)
        scores = torch.where(mask[None, None], scores, -1e9)
        p = _softmax(scores, axis=-1)
        o = torch.einsum("bhqk,bhkd->bhqd", p, v)
        return (lambda x, *d: x.permute(*d))(o, 0, 2, 1, 3).reshape(B, S, D) @ wo

    def _B_ref(x, w_gate, w_up, w_down):
        return (_gelu(x @ w_gate) * (x @ w_up)) @ w_down

    x = A_x
    h = x + _A_ref(_layernorm(x, axis=-1), A_wqkv, A_wo)
    h2 = h + _B_ref(_layernorm(h, axis=-1), B_w_gate, B_w_up, B_w_down)
    return h2
```

```cpp
#include <hip/hip_runtime.h>
#include <hip/hip_bf16.h>
typedef unsigned short u16;
__device__ __forceinline__ float bf2f(u16 b){ return __uint_as_float(((unsigned)b)<<16); }
__device__ __forceinline__ u16 f2bf(float f){ unsigned u=__float_as_uint(f); u += 0x7fffu + ((u>>16)&1u); return (u16)(u>>16); }
constexpr int S=2048, D=640, H=32, DH=20, W=256, F=1728;

__global__ void ln_kernel(const u16* x, u16* y){
  int row = blockIdx.x; int lane = threadIdx.x;
  float v[10]; float s=0;
  for(int i=0;i<10;i++){ v[i]=bf2f(x[row*D+lane+64*i]); s+=v[i]; }
  for(int o=32;o>0;o>>=1) s+=__shfl_xor(s,o);
  float m=s/D; float q=0;
  for(int i=0;i<10;i++){ float d=v[i]-m; q+=d*d; }
  for(int o=32;o>0;o>>=1) q+=__shfl_xor(q,o);
  float r=rsqrtf(q/D+1e-5f);
  for(int i=0;i<10;i++) y[row*D+lane+64*i]=f2bf((v[i]-m)*r);
}
__global__ void gemm_kernel(const u16* A, const u16* B, const u16* R, u16* C, int M,int N,int K){
  int n = blockIdx.x*64+threadIdx.x; int m = blockIdx.y;
  if(n>=N) return;
  float acc=0;
  for(int k=0;k<K;k++) acc += bf2f(A[m*K+k])*bf2f(B[k*N+n]);
  if(R) acc = bf2f(f2bf(acc)) + bf2f(R[m*N+n]);
  C[m*N+n]=f2bf(acc);
}
__global__ void attn_kernel(const u16* qkv, u16* o){
  int q = blockIdx.x; int h = threadIdx.x;
  float qv[DH]; for(int d=0;d<DH;d++) qv[d]=bf2f(qkv[q*3*D+h*DH+d]);
  int k0 = q-W+1; if(k0<0)k0=0;
  float m=-1e30f;
  for(int k=k0;k<=q;k++){ float s=0; for(int d=0;d<DH;d++) s+=qv[d]*bf2f(qkv[k*3*D+D+h*DH+d]); s*=0.22360679774997896f; m=fmaxf(m,s);}
  float l=0; float acc[DH]; for(int d=0;d<DH;d++)acc[d]=0;
  for(int k=k0;k<=q;k++){ float s=0; for(int d=0;d<DH;d++) s+=qv[d]*bf2f(qkv[k*3*D+D+h*DH+d]); s*=0.22360679774997896f; float p=__expf(s-m); l+=p; for(int d=0;d<DH;d++) acc[d]+=p*bf2f(qkv[k*3*D+2*D+h*DH+d]); }
  for(int d=0;d<DH;d++) o[q*D+h*DH+d]=f2bf(acc[d]/l);
}
__global__ void geglu_kernel(const u16* g, const u16* u, u16* a, int n){
  int i=blockIdx.x*256+threadIdx.x; if(i>=n) return;
  float x=bf2f(g[i]); float t=tanhf(0.7978845608028654f*(x+0.044715f*x*x*x));
  float ge=0.5f*x*(1.f+t);
  a[i]=f2bf(bf2f(f2bf(ge))*bf2f(u[i]));
}
extern "C" void kernel_launch(void* const* d_in, const int* in_sizes, int n_in,
                              void* d_out, int out_size, void* d_ws, size_t ws_size,
                              hipStream_t stream) {
  const u16* x=(const u16*)d_in[0]; const u16* wqkv=(const u16*)d_in[1]; const u16* wo=(const u16*)d_in[2];
  const u16* wg=(const u16*)d_in[4]; const u16* wu=(const u16*)d_in[5]; const u16* wd=(const u16*)d_in[6];
  u16* ws=(u16*)d_ws;
  u16* xn=ws; u16* qkv=xn+S*D; u16* o=qkv+S*3*D; u16* h=o+S*D; u16* hn=h+S*D; u16* g=hn+S*D; u16* u=g+S*F; u16* a=u+S*F;
  ln_kernel<<<S,64,0,stream>>>(x,xn);
  gemm_kernel<<<dim3(3*D/64,S),64,0,stream>>>(xn,wqkv,nullptr,qkv,S,3*D,D);
  attn_kernel<<<S,32,0,stream>>>(qkv,o);
  gemm_kernel<<<dim3(D/64,S),64,0,stream>>>(o,wo,x,h,S,D,D);
  ln_kernel<<<S,64,0,stream>>>(h,hn);
  gemm_kernel<<<dim3(F/64,S),64,0,stream>>>(hn,wg,nullptr,g,S,F,D);
  gemm_kernel<<<dim3(F/64,S),64,0,stream>>>(hn,wu,nullptr,u,S,F,D);
  geglu_kernel<<<(S*F+255)/256,256,0,stream>>>(g,u,a,S*F);
  gemm_kernel<<<dim3(D/64,S),64,0,stream>>>(a,wd,h,(u16*)d_out,S,D,F);
}
```

```cpp
#include <hip/hip_runtime.h>
#include <stdint.h>

typedef __bf16 bf16x8 __attribute__((ext_vector_type(8)));
typedef __bf16 bf16x4 __attribute__((ext_vector_type(4)));
typedef __bf16 bf16x2 __attribute__((ext_vector_type(2)));
typedef float f32x16 __attribute__((ext_vector_type(16)));
typedef float f32x4 __attribute__((ext_vector_type(4)));
typedef float f32x2 __attribute__((ext_vector_type(2)));
typedef unsigned u32x4 __attribute__((ext_vector_type(4)));
typedef unsigned u32x2 __attribute__((ext_vector_type(2)));
typedef unsigned long long u64;
#define LAS __attribute__((address_space(3)))
#define GAS __attribute__((address_space(1)))
#define DI __device__ __forceinline__
typedef __amdgpu_buffer_rsrc_t rsrc_t;

constexpr int OFF_EPOCH = 0;
constexpr int OFF_FP = 1024;
constexpr int OFF_XST = 4096;
constexpr int OFF_FG = 20480;
constexpr int OFF_FW = 28672;
constexpr int OFF_F1 = 32768;
constexpr int OFF_F2 = 65536;
constexpr int OFF_F3 = 98304;
constexpr int OFF_F4 = 131072;
constexpr int OFF_HST = 262144;
constexpr int OFF_QKV = 524288;
constexpr int OFF_OF = OFF_QKV + 3 * 16 * 2048 * 80;
constexpr int OFF_HF = OFF_OF + 2048 * 640 * 2;
constexpr int OFF_ACT = OFF_HF + 2048 * 640 * 2;
constexpr int OFF_WGU = OFF_ACT + 2048 * 1728 * 2;
constexpr int OFF_WO = OFF_WGU + 108 * 40 * 1024;
constexpr int OFF_WD = OFF_WO + 40 * 20 * 1024;
constexpr int WS_END = OFF_WD + 40 * 54 * 1024;
constexpr int OFF_DBG = WS_END;
constexpr int WS_BYTES = WS_END + 256 * 512 * 4;
constexpr int LDS_BYTES = 163840;
constexpr int SC1 = 16;
constexpr int OOB = 0x70000000;

struct Params {
  const void* x; const void* wqkv; const void* wo; const void* wg; const void* wu; const void* wd;
  void* out; void* ws;
};

struct Buf { rsrc_t r; u32x4 q; };
DI Buf mk_rsrc(const void* p, int bytes) {
  Buf b; b.r = __builtin_amdgcn_make_buffer_rsrc((void*)p, 0, bytes, 0x00020000);
  const u64 a = (u64)p; b.q = u32x4{(unsigned)a, (unsigned)(a >> 32) & 0xffffu, (unsigned)bytes, 0x00020000u};
  return b;
}
DI unsigned lds_addr(const char* p) { return (unsigned)(uintptr_t)(const LAS char*)p; }
template <int AUX> DI void dma16(const Buf& b, char* lds_dst, int voff, int soff) {
  if constexpr (AUX == 0)
    asm volatile("s_mov_b32 m0, %0\n\ts_nop 0\n\tbuffer_load_dwordx4 %1, %2, %3 offen lds" ::"s"(lds_addr(lds_dst)), "v"(voff), "s"(b.q), "s"(soff) : "memory");
  else
    asm volatile("s_mov_b32 m0, %0\n\ts_nop 0\n\tbuffer_load_dwordx4 %1, %2, %3 offen sc1 lds" ::"s"(lds_addr(lds_dst)), "v"(voff), "s"(b.q), "s"(soff) : "memory");
}
DI void dma4(const Buf& b, char* lds_dst, int voff, int soff) {
  asm volatile("s_mov_b32 m0, %0\n\ts_nop 0\n\tbuffer_load_dword %1, %2, %3 offen lds" ::"s"(lds_addr(lds_dst)), "v"(voff), "s"(b.q), "s"(soff) : "memory");
}
template <int AUX> DI u32x4 hld16(const Buf& b, int voff, int soff = 0) {
  u32x4 r;
  if constexpr (AUX == 0) asm volatile("buffer_load_dwordx4 %0, %1, %2, %3 offen" : "=v"(r) : "v"(voff), "s"(b.q), "s"(soff) : "memory");
  else asm volatile("buffer_load_dwordx4 %0, %1, %2, %3 offen sc1" : "=v"(r) : "v"(voff), "s"(b.q), "s"(soff) : "memory");
  return r;
}
template <int AUX> DI u32x2 hld8(const Buf& b, int voff) {
  u32x2 r;
  if constexpr (AUX == 0) asm volatile("buffer_load_dwordx2 %0, %1, %2, 0 offen" : "=v"(r) : "v"(voff), "s"(b.q) : "memory");
  else asm volatile("buffer_load_dwordx2 %0, %1, %2, 0 offen sc1" : "=v"(r) : "v"(voff), "s"(b.q) : "memory");
  return r;
}
template <int AUX> DI unsigned hld4(const Buf& b, int voff) {
  unsigned r;
  if constexpr (AUX == 0) asm volatile("buffer_load_dword %0, %1, %2, 0 offen" : "=v"(r) : "v"(voff), "s"(b.q) : "memory");
  else asm volatile("buffer_load_dword %0, %1, %2, 0 offen sc1" : "=v"(r) : "v"(voff), "s"(b.q) : "memory");
  return r;
}
template <int AUX> DI u32x4 ld16(const Buf& rs, int voff, int soff = 0) { return __builtin_amdgcn_raw_buffer_load_b128(rs.r, voff, soff, AUX); }
template <int AUX> DI u32x2 ld8(const Buf& rs, int voff, int soff = 0) { return __builtin_amdgcn_raw_buffer_load_b64(rs.r, voff, soff, AUX); }
template <int AUX> DI void st16(const Buf& rs, u32x4 v, int voff) { __builtin_amdgcn_raw_buffer_store_b128(v, rs.r, voff, 0, AUX); }
template <int AUX> DI void st8(const Buf& rs, u32x2 v, int voff, int soff = 0) { __builtin_amdgcn_raw_buffer_store_b64(v, rs.r, voff, soff, AUX); }
DI bf16x8 as_frag(u32x4 v) { return __builtin_bit_cast(bf16x8, v); }
DI bf16x8 lds_frag(const char* p) { return *(const LAS bf16x8*)(p); }
DI bf16x4 lds_tr(const char* p) { return __builtin_amdgcn_ds_read_tr16_b64_v4bf16((LAS bf16x4*)(p)); }
DI bf16x8 cat(bf16x4 a, bf16x4 b) { return __builtin_shufflevector(a, b, 0, 1, 2, 3, 4, 5, 6, 7); }
DI unsigned pk2(float lo, float hi) { f32x2 v = {lo, hi}; return __builtin_bit_cast(unsigned, __builtin_convertvector(v, bf16x2)); }
DI float rbf(float v) { return __uint_as_float(pk2(v, v) << 16); }
DI float bflo(unsigned u) { return __uint_as_float(u << 16); }
DI float bfhi(unsigned u) { return __uint_as_float(u & 0xffff0000u); }
DI float dot2(unsigned a, unsigned b, float c) {
  return __builtin_amdgcn_fdot2_f32_bf16(__builtin_bit_cast(bf16x2, a), __builtin_bit_cast(bf16x2, b), c, false);
}
DI float sfma(float a, float b, float c) { float d; asm("v_fma_f32 %0, %1, %2, %3" : "=v"(d) : "v"(a), "v"(b), "v"(c)); return d; }
DI float sadd(float a, float b) { float d; asm("v_add_f32 %0, %1, %2" : "=v"(d) : "v"(a), "v"(b)); return d; }
DI float smul(float a, float b) { float d; asm("v_mul_f32 %0, %1, %2" : "=v"(d) : "v"(a), "v"(b)); return d; }
DI float sfma_t(float a, float b, float c) { float d; asm("s_nop 0\n\tv_fma_f32 %0, %1, %2, %3" : "=v"(d) : "v"(a), "v"(b), "v"(c)); return d; }
DI float smul_t(float a, float b) { float d; asm("s_nop 0\n\tv_mul_f32 %0, %1, %2" : "=v"(d) : "v"(a), "v"(b)); return d; }
#define MFMA32(a, b, c) __builtin_amdgcn_mfma_f32_32x32x16_bf16((a), (b), (c), 0, 0, 0)
#define MFMA16(a, b, c) __builtin_amdgcn_mfma_f32_16x16x32_bf16((a), (b), (c), 0, 0, 0)
#define WAITV(n) asm volatile("s_waitcnt vmcnt(" #n ")" ::: "memory")
#define WAITL() asm volatile("s_waitcnt lgkmcnt(0)" ::: "memory")
#define SB() __builtin_amdgcn_sched_barrier(0)
#define BAR() do { asm volatile("s_waitcnt lgkmcnt(0)\n\ts_barrier" ::: "memory"); } while (0)
constexpr unsigned ONES2 = 0x3f803f80u;

DI float xhalf(float v) { return __shfl_xor(v, 32); }
DI void set_flag(void* ws, int off, unsigned tag) {
  __hip_atomic_store((GAS unsigned*)((GAS char*)ws + off), tag, __ATOMIC_RELAXED, __HIP_MEMORY_SCOPE_AGENT);
}
DI void wait_flags(void* ws, int off, unsigned tag) {
  GAS unsigned* p = (GAS unsigned*)((GAS char*)ws + off);
  for (int spin = 0; spin < (1 << 17); ++spin) {
    unsigned v = __hip_atomic_load(p, __ATOMIC_RELAXED, __HIP_MEMORY_SCOPE_AGENT);
    if (__all(v == tag)) break;
    __builtin_amdgcn_s_sleep(1);
  }
  __builtin_amdgcn_fence(__ATOMIC_ACQUIRE, "wavefront");
}
template <int CTRL> DI float dpp_add(float v) { return v + __builtin_bit_cast(float, __builtin_amdgcn_update_dpp(0, __builtin_bit_cast(int, v), CTRL, 0xf, 0xf, true)); }
DI float wave_sum(float v) {
  v = dpp_add<0xB1>(v); v = dpp_add<0x4E>(v); v = dpp_add<0x141>(v); v = dpp_add<0x140>(v);
  v += __shfl_xor(v, 16); v += __shfl_xor(v, 32);
  return v;
}
DI void ln_stats(float sum, float sq, float& mean, float& rstd) {
  float m = sum * (1.0f / 640.0f);
  float var = fmaxf(sq * (1.0f / 640.0f) - m * m, 0.0f);
  mean = rbf(m);
  float s = rbf(__builtin_amdgcn_sqrtf(rbf(rbf(var) + 1e-5f)));
  rstd = __builtin_amdgcn_rcpf(s);
}

#define STAMP(i) do {} while (0)
#define STAMPC(i) do {} while (0)
#define STAMP_DECL do {} while (0)
#define STAMP_FLUSH() do {} while (0)

template <bool EDGE, class F, class G>
DI void attn_body(char* lds, int KB, int VB, int ONE, int hh, int qt, int i0, int lane, bf16x8 q0, bf16x8 q1, f32x16& O, F vwait, G mid) {
  const int l31 = lane & 31, h = lane >> 5;
  const char* kp0 = lds + KB + (32 * qt + l31) * 80 + ((hh ? 3 : 0) + h) * 16;
  const char* kp1 = lds + KB + (32 * qt + l31) * 80 + 32;
  constexpr float NEG = -1e30f;
  f32x16 S[8];
  bf16x8 k0[9], k1[9];
  constexpr int ord[9] = {8, 0, 1, 2, 3, 4, 5, 6, 7};
#pragma unroll
  for (int n = 0; n < 3; ++n) { k0[n] = lds_frag(kp0 + ord[n] * 2560); k1[n] = lds_frag(kp1 + ord[n] * 2560); }
  float m0 = NEG, m1 = NEG;
  f32x16 D;
#pragma unroll
  for (int n = 0; n < 9; ++n) {
    if (n + 3 < 9) { k0[n + 3] = lds_frag(kp0 + ord[n + 3] * 2560); k1[n + 3] = lds_frag(kp1 + ord[n + 3] * 2560); }
    f32x16 z = {};
    z = MFMA32(k0[n], q0, z);
    z = MFMA32(k1[n], q1, z);
    if (n == 0) D = z; else S[n - 1] = z;
    if (n >= 2) {
      const int i = n - 2;
      if (EDGE) {
#pragma unroll
        for (int r = 0; r < 16; ++r) S[i][r] = (i < i0) ? NEG : S[i][r];
      }
      if (i == 0) {
#pragma unroll
        for (int r = 0; r < 16; ++r) S[0][r] = ((r & 3) + 8 * (r >> 2) + 4 * h <= l31) ? D[r] : S[0][r];
      }
#pragma unroll
      for (int r = 0; r < 16; r += 4) { m0 = fmaxf(fmaxf(m0, S[i][r]), S[i][r + 1]); m1 = fmaxf(fmaxf(m1, S[i][r + 2]), S[i][r + 3]); }
      asm volatile("" : "+v"(m0), "+v"(m1));
    }
    SB();
  }
  if (EDGE) {
#pragma unroll
    for (int r = 0; r < 16; ++r) S[7][r] = (7 < i0) ? NEG : S[7][r];
  }
#pragma unroll
  for (int r = 0; r < 16; r += 4) { m0 = fmaxf(fmaxf(m0, S[7][r]), S[7][r + 1]); m1 = fmaxf(fmaxf(m1, S[7][r + 2]), S[7][r + 3]); }
  float m = fmaxf(m0, m1);
  m = fmaxf(m, xhalf(m));
  constexpr float C = 0.22360679774997896f * 1.4426950408889634f;
  const f32x2 C2 = {C, C}, mc2 = {-m * C, -m * C};
  const int gi = lane >> 4, dh = gi & 1, q = (lane & 15) >> 2, p = lane & 3;
  const bool one = dh && (p == 1 || p == 2);
  const int vbase = one ? ONE : (VB + (32 * qt + 4 * h + q) * 80 + hh * 40 + dh * 32 + 8 * p);
  const int vstr = one ? 0 : 640;
  vwait();
  auto vfrag = [&](int i, int s) { return cat(lds_tr(lds + vbase + vstr * (4 * i + 2 * s)), lds_tr(lds + vbase + vstr * (4 * i + 2 * s + 1))); };
  O = f32x16{};
  bf16x8 vf[2][2];
  vf[1][0] = vfrag(1, 0); vf[1][1] = vfrag(1, 1);
#pragma unroll
  for (int n = 1; n <= 8; ++n) {
    const int i = n & 7;
    if (n == 6) mid();
    if (n < 8) { vf[(n + 1) & 1][0] = vfrag((n + 1) & 7, 0); vf[(n + 1) & 1][1] = vfrag((n + 1) & 7, 1); }
    else { vf[1][0] = vfrag(8, 0); vf[1][1] = vfrag(8, 1); }
#pragma unroll
    for (int s = 0; s < 2; ++s) {
      u32x4 pk;
#pragma unroll
      for (int j = 0; j < 4; ++j) {
        f32x2 a = {S[i][8 * s + 2 * j], S[i][8 * s + 2 * j + 1]};
        a = __builtin_elementwise_fma(a, C2, mc2);
        pk[j] = pk2(__builtin_amdgcn_exp2f(a.x), __builtin_amdgcn_exp2f(a.y));
      }
      if (n < 8) O = MFMA32(vf[n & 1][s], as_frag(pk), O);
      else {
        u32x4 pd, po;
#pragma unroll
        for (int j = 0; j < 4; ++j) {
          const int r = 8 * s + 2 * j, kk = (r & 3) + 8 * (r >> 2) + 4 * h;
          const unsigned mk = (kk <= l31 ? 0xffffu : 0u) | (kk + 1 <= l31 ? 0xffff0000u : 0u);
          pd[j] = pk[j] & mk; po[j] = pk[j] & ~mk;
        }
        O = MFMA32(vf[0][s], as_frag(po), O);
        O = MFMA32(vf[1][s], as_frag(pd), O);
      }
    }
    SB();
  }
}

__global__ void __launch_bounds__(512, 2) fused_block(Params P) {
  extern __shared__ __attribute__((aligned(16))) char lds[];
  const int tid = threadIdx.x, lane = tid & 63;
  const int w = __builtin_amdgcn_readfirstlane(tid >> 6);
  const int bid = blockIdx.x, xcd = bid & 7, slot = bid >> 3;
  const int l31 = lane & 31, h = lane >> 5, l15 = lane & 15, g = lane >> 4;
  const int q4 = l15 >> 2, p4 = lane & 3, r8 = lane >> 3, c8 = (lane & 7) * 16;
  const Buf rws = mk_rsrc(P.ws, WS_BYTES);
  const Buf rx = mk_rsrc(P.x, 2048 * 640 * 2);
  STAMP_DECL;
  STAMP(0);
  unsigned epoch = hld4<SC1>(rws, OFF_EPOCH);
  unsigned tag;
  const int panel1 = 4 * (xcd >> 1) + (slot & 3), ct = 8 * (xcd & 1) + (slot >> 2);
  u32x4 pkf[4];
  int pk_dst = OOB, pk_flag = -1;
  const int srow = panel1 * 128 + ct * 8 + w;
  u32x4 sx4 = hld16<0>(rx, srow * 1280 + lane * 20);
  unsigned sx1 = hld4<0>(rx, srow * 1280 + lane * 20 + 16);
  auto xstats = [&]() {
    float v[10] = {bflo(sx4[0]), bfhi(sx4[0]), bflo(sx4[1]), bfhi(sx4[1]), bflo(sx4[2]), bfhi(sx4[2]), bflo(sx4[3]), bfhi(sx4[3]), bflo(sx1), bfhi(sx1)};
    float sum = 0.f;
#pragma unroll
    for (int i = 0; i < 10; ++i) sum += v[i];
    const float m = wave_sum(sum) * (1.0f / 640.0f);
    float sq = 0.f;
#pragma unroll
    for (int i = 0; i < 10; ++i) sq += (v[i] - m) * (v[i] - m);
    const float var = wave_sum(sq) * (1.0f / 640.0f);
    const float sd = __builtin_amdgcn_sqrtf(rbf(rbf(var) + 1e-5f));
    tag = __builtin_amdgcn_readfirstlane(epoch * 1664525u + 1013904223u);
    st8<SC1>(rws, u32x2{pk2(m, sd), tag}, lane == 0 ? OFF_XST + srow * 8 : OOB);
  };

  if (ct < 15) {
    const int panel = panel1;
    const Buf rw = mk_rsrc(P.wqkv, 640 * 1920 * 2);
    constexpr int XS = 0, WSB = 65536, ST = 131072, CSP = ST + 2048, CSF = CSP + 2048, TCH = CSF + 1024;
    const int xv0 = (panel * 128 + 16 * w + r8) * 1280 + ((lane & 7) ^ (r8 >> 1)) * 16;
    const int xv1 = (panel * 128 + 16 * w + 8 + r8) * 1280 + ((lane & 7) ^ (4 + (r8 >> 1))) * 16;
    const int wv = (8 * w + g) * 3840 + ct * 256 + (((q4 ^ g) << 2) | p4) * 16;
    const int rot = (3 * (slot & 3) + (slot >> 2) + 5 * (xcd >> 2)) % 10;
    auto issue1 = [&](int t, int i) {
      char* xs = lds + XS + (t & 3) * 16384 + w * 2048;
      char* ws_ = lds + WSB + (t & 3) * 16384 + w * 2048;
      const int kt = t + rot >= 10 ? t + rot - 10 : t + rot;
      if (i == 0) dma16<0>(rx, xs, xv0, kt * 128);
      if (i == 1) dma16<0>(rw, ws_, wv, kt * 245760);
      if (i == 2) dma16<0>(rx, xs + 1024, xv1, kt * 128);
      if (i == 3) dma16<0>(rw, ws_ + 1024, wv, kt * 245760 + 15360);
    };
    auto issue = [&](int t) { issue1(t, 0); issue1(t, 1); issue1(t, 2); issue1(t, 3); };
#define P1_AHEAD 2
    constexpr int AH = P1_AHEAD;
    issue(0); __builtin_amdgcn_s_barrier(); issue(1);
    if (AH > 2) { __builtin_amdgcn_s_barrier(); issue(2); }
    STAMP(23);
    const int tt = w & 3, nh = w >> 2, f = tt ^ (2 * nh);
    const int row = 32 * tt + l31;
    const int e = (2 * f) ^ h ^ ((row >> 1) & 7);
    const char* xa[4]; const char* wa[4][2];
#pragma unroll
    for (int s = 0; s < 4; ++s) {
      xa[s] = lds + XS + row * 128 + (((2 * s) ^ e) * 16);
#pragma unroll
      for (int j = 0; j < 2; ++j)
        wa[s][j] = lds + WSB + (16 * (s ^ f) + 8 * h + q4) * 256 + (((2 * nh + j) ^ q4) * 64) + 32 * (g & 1) + 8 * p4;
    }
    const int sec = ct / 5, c0 = (ct % 5) * 128, hp0 = c0 / 40;
    f32x16 acc[2] = {};
    float cs[2] = {0.f, 0.f};
    bf16x8 xf[2][4], wf[2][4][2];
    if (AH > 2) asm volatile("s_waitcnt vmcnt(12)" : "+v"(epoch), "+v"(sx4), "+v"(sx1) :: "memory");
    else asm volatile("s_waitcnt vmcnt(8)" : "+v"(epoch), "+v"(sx4), "+v"(sx1) :: "memory");
    STAMP(3);
    xstats();
    u32x2 xs; u32x4 src[4];
    const int u = (bid < 224 ? bid : 112 + (bid >> 1)) * 5 + w;
    const bool dopk = w < 5 && u < 1080;
    const int pcb = u % 27, pks = u / 27;
#pragma unroll
    for (int t = 0; t <= 10; ++t) {
      if (t < 10) {
        if (AH > 2) { if (t < 3) WAITV(9); else if (t < 8) WAITV(8); else if (t == 8) WAITV(9); else WAITV(5); }
        else { if (t < 2) WAITV(5); else if (t < 9) WAITV(4); else WAITV(5); }
        BAR();
        if (t == 0) STAMP(24);
      }
      if (t == 10 - AH) {
        xs = hld8<SC1>(rws, OFF_XST + (panel * 128 + row) * 8);
        const Buf rg = mk_rsrc(P.wg, 640 * 1728 * 2), ru = mk_rsrc(P.wu, 640 * 1728 * 2);
        const int v = dopk ? (16 * pks + r8) * 3456 + pcb * 128 + c8 : OOB;
        src[0] = hld16<0>(rg, v); src[1] = hld16<0>(rg, v, 8 * 3456); src[2] = hld16<0>(ru, v); src[3] = hld16<0>(ru, v, 8 * 3456);
      }
      SB();
      const int so = (t & 3) * 16384, b = t & 1;
#pragma unroll
      for (int s = 0; s < 4; ++s) {
        if (t > 0) {
          acc[0] = MFMA32(wf[b ^ 1][s][0], xf[b ^ 1][s], acc[0]);
          acc[1] = MFMA32(wf[b ^ 1][s][1], xf[b ^ 1][s], acc[1]);
        }
        if (t < 10) {
          xf[b][s] = lds_frag(xa[s] + so);
          wf[b][s][0] = cat(lds_tr(wa[s][0] + so), lds_tr(wa[s][0] + so + 1024));
          wf[b][s][1] = cat(lds_tr(wa[s][1] + so), lds_tr(wa[s][1] + so + 1024));
        }
        if (t > 0) {
          const unsigned a = __builtin_bit_cast(u32x4, wf[b ^ 1][3][0])[s], c = __builtin_bit_cast(u32x4, wf[b ^ 1][3][1])[s];
          cs[0] = sadd(sadd(cs[0], bflo(a)), bfhi(a)); cs[1] = sadd(sadd(cs[1], bflo(c)), bfhi(c));
          asm volatile("" : "+v"(cs[0]), "+v"(cs[1]));
        }
        SB();
        if (t + AH < 10) issue1(t + AH, s);
        SB();
      }
    }
    STAMP(1);
    cs[0] += xhalf(cs[0]); cs[1] += xhalf(cs[1]);
    if (lane < 32) {
      *(LAS float*)(lds + CSP + (tt * 128 + 64 * nh + l31) * 4) = cs[0];
      *(LAS float*)(lds + CSP + (tt * 128 + 64 * nh + 32 + l31) * 4) = cs[1];
    }
    BAR();
    if (tid < 128) {
      const LAS float* c = (const LAS float*)(lds + CSP) + tid;
      *(LAS float*)(lds + CSF + tid * 4) = (c[0] + c[128]) + (c[256] + c[384]);
    }
    asm volatile("s_waitcnt vmcnt(4)" : "+v"(xs) :: "memory");
    for (int spin = 0; spin < (1 << 17) && !__all(xs[1] == tag); ++spin) {
      __builtin_amdgcn_s_sleep(1);
      asm volatile("" ::: "memory");
      xs = ld8<SC1>(rws, OFF_XST + (panel * 128 + row) * 8);
    }
    const float mean = bflo(xs[0]), rstd = __builtin_amdgcn_rcpf(bfhi(xs[0]));
    BAR();
    {
      f32x4 c[8];
#pragma unroll
      for (int i = 0; i < 8; ++i) c[i] = *(const LAS f32x4*)(lds + CSF + (64 * nh + 8 * i + 4 * h) * 4);
      SB();
#pragma unroll
      for (int i = 0; i < 8; ++i) {
        const int j = i >> 2, gq = i & 3;
        float v0 = (acc[j][4 * gq + 0] - mean * c[i][0]) * rstd, v1 = (acc[j][4 * gq + 1] - mean * c[i][1]) * rstd;
        float v2 = (acc[j][4 * gq + 2] - mean * c[i][2]) * rstd, v3 = (acc[j][4 * gq + 3] - mean * c[i][3]) * rstd;
        const int gb = c0 - hp0 * 40 + 64 * nh + 8 * i, b = gb / 40, cc = gb - b * 40;
        *(LAS u32x2*)(lds + b * 10240 + cc * 2 + row * 80 + 8 * h) = u32x2{pk2(v0, v1), pk2(v2, v3)};
      }
    }
    BAR();
    STAMP(22);
    {
      u32x4 v[5];
#pragma unroll
      for (int i = 0; i < 5; ++i) v[i] = *(const LAS u32x4*)(lds + (w * 5 + i) * 1024 + lane * 16);
#pragma unroll
      for (int i = 0; i < 5; ++i) {
        const int b = w >> 1, ci = ((w & 1) * 5 + i) * 64 + lane;
        const int col = 40 * (hp0 + b) + 8 * (ci % 5);
        const bool own = col >= c0 && col < c0 + 128;
        st16<SC1>(rws, v[i], own ? ci * 16 + OFF_QKV + ((sec * 16 + hp0 + b) * 2048 + panel * 128) * 80 : OOB);
      }
    }
    SB();
    {
      char* sc = lds + 40960 + (w < 5 ? w : 0) * 4096;
      asm volatile("s_waitcnt vmcnt(5)" : "+v"(src[0]), "+v"(src[1]), "+v"(src[2]), "+v"(src[3]) :: "memory");
      if (w < 5) {
#pragma unroll
        for (int i = 0; i < 4; ++i) *(LAS u32x4*)(sc + i * 1024 + lane * 16) = src[i];
      }
      WAITL();
      const char* a = sc + (g & 1) * 2048 + (8 * h + q4) * 128 + 8 * p4;
#pragma unroll
      for (int i = 0; i < 4; ++i) pkf[i] = __builtin_bit_cast(u32x4, cat(lds_tr(a + 32 * i), lds_tr(a + 32 * i + 512)));
      pk_dst = dopk ? OFF_WGU + (4 * pcb * 40 + pks) * 1024 + lane * 16 : OOB;
      pk_flag = dopk ? OFF_FG + (pcb * 64 + pks) * 4 : -1;
    }
    WAITV(0);
    BAR();
    if (tid == 0) set_flag(P.ws, OFF_F1 + (panel * 16 + ct) * 128, tag);
  } else {
    const Buf ro_ = mk_rsrc(P.wo, 640 * 640 * 2), rd_ = mk_rsrc(P.wd, 1728 * 640 * 2);
    const int rank = ((xcd >> 1) * 4 + (slot & 3)) * 8 + w;
    char* sc = lds + w * 16384;
    const int jr = 8 * (r8 >> 2) + (r8 & 3);
    auto pk_issue = [&](int k) {
      const int u = rank + 128 * k;
      const bool iswo = u < 200;
      const int v = iswo ? u : u - 200, cb = v % 10, s = v / 10;
      char* d = sc + (k & 3) * 4096;
      if (iswo) {
        const int vo = (s < 16 ? jr : 20 * (r8 >> 2) + (r8 & 3)) * 1280 + c8;
        const int b0 = (s < 16 ? 40 * s : 160 * (s - 16) + 16) * 1280 + cb * 128, d1 = (s < 16 ? 4 : 40) * 1280, d2 = (s < 16 ? 20 : 80) * 1280;
        dma16<0>(ro_, d, vo, b0); dma16<0>(ro_, d + 1024, vo, b0 + d1); dma16<0>(ro_, d + 2048, vo, b0 + d2); dma16<0>(ro_, d + 3072, vo, b0 + d1 + d2);
      } else {
        const int vo = u < 740 ? jr * 1280 + c8 : OOB, b0 = 32 * s * 1280 + cb * 128;
        dma16<0>(rd_, d, vo, b0); dma16<0>(rd_, d + 1024, vo, b0 + 4 * 1280); dma16<0>(rd_, d + 2048, vo, b0 + 16 * 1280); dma16<0>(rd_, d + 3072, vo, b0 + 20 * 1280);
      }
    };
    pk_issue(0); pk_issue(1); pk_issue(2);
    asm volatile("s_waitcnt vmcnt(12)" : "+v"(epoch), "+v"(sx4), "+v"(sx1) :: "memory");
    xstats();
#pragma unroll 1
    for (int k = 0; k < 6; ++k) {
      if (k + 3 < 6) pk_issue(k + 3);
      if (k < 3) WAITV(12); else WAITV(4);
      if (k == 3) { BAR(); if (tid == 0) set_flag(P.ws, OFF_FW + panel1 * 128, tag); }
      const int u = rank + 128 * k;
      const bool iswo = u < 200;
      const int v = iswo ? u : u - 200, cb = v % 10, s = v / 10;
      const char* a = sc + (k & 3) * 4096 + (8 * g + q4) * 128 + 8 * p4;
#pragma unroll
      for (int i = 0; i < 4; ++i) {
        bf16x8 fr = cat(lds_tr(a + 32 * i), lds_tr(a + 32 * i + 512));
        const int nt = 4 * cb + i, m = nt / 5, j = nt - 5 * m;
        int dst;
        if (iswo) dst = OFF_WO + (nt * 20 + s) * 1024 + lane * 16;
        else if (j == 2) dst = OFF_WD + ((2 * m + (l15 >> 3)) * 54 + s) * 2560 + 2048 + g * 128 + (l15 & 7) * 16;
        else dst = OFF_WD + ((2 * m + (j > 2)) * 54 + s) * 2560 + (j > 2 ? j - 3 : j) * 1024 + lane * 16;
        st16<SC1>(rws, __builtin_bit_cast(u32x4, fr), u < 740 ? dst : OOB);
      }
    }
#pragma unroll
    for (int i = 0; i < 4; ++i) pkf[i] = u32x4{};
    WAITV(0);
    BAR();
    if (tid == 0) set_flag(P.ws, OFF_F1 + (panel1 * 16 + 15) * 128, tag);
  }
  STAMP(2);

  constexpr int KB = 0, VB = 30720, WOB = 61440, MISC = 161792;
  const int tp3 = 4 * xcd + (slot & 3), cg3 = slot >> 2;
  {
    const int panel = panel1, hp = ct;
    wait_flags(P.ws, OFF_FW + l15 * 128, tag);
#pragma unroll
    for (int i = 0; i < 12; ++i) {
      const int pc = min(w + 8 * i, 94), nt = pc / 19, s = pc - nt * 19;
      dma16<SC1>(rws, lds + WOB + pc * 1024, lane * 16, OFF_WO + ((5 * cg3 + nt) * 20 + s) * 1024);
    }
    if (w == 1) {
      const int ctA = (40 * hp) >> 7, ctB = (40 * hp + 39) >> 7;
      const int i = lane < 14 ? lane : 0;
      const int sec = i < 2 ? 0 : (i < 8 ? 1 : 2);
      const int rem = i < 2 ? i : (i < 8 ? i - 2 : i - 8);
      int pp = panel - (rem >> 1); pp = pp < 0 ? panel : pp;
      wait_flags(P.ws, OFF_F1 + (pp * 16 + sec * 5 + ((rem & 1) ? ctB : ctA)) * 128, tag);
    }
    if (tid == 0) *(LAS u32x2*)(lds + MISC) = u32x2{0x00003f80u, 0u};
    BAR();
    STAMP(4);
    const int hh = w >> 2, qt = w & 3;
    const int tok0 = 128 * (panel - 2);
    const int qoff = OFF_QKV + (hp * 2048 + 128 * panel + 32 * qt + l31) * 80;
    u32x4 q0 = hld16<SC1>(rws, qoff + ((hh ? 3 : 0) + h) * 16);
    u32x4 q1 = hld16<SC1>(rws, h ? OOB : qoff + 32);
#pragma unroll
    for (int i = 0; i < 8; ++i) {
      const int isv = i >> 2, pi = min(w + 8 * (i & 3), 29);
      const int rel = tok0 * 80 + pi * 1024;
      dma16<SC1>(rws, lds + isv * VB + pi * 1024, rel < 0 ? OOB : lane * 16, OFF_QKV + ((1 + isv) * 16 + hp) * 2048 * 80 + (rel < 0 ? 0 : rel));
    }
    asm volatile("s_waitcnt vmcnt(4)" : "+v"(q0), "+v"(q1) :: "memory");
    if (hh) { q1[0] = 0; q1[1] = 0; } else { q1[2] = 0; q1[3] = 0; }
    BAR();
    STAMP(5);
    auto vwait = [&]() {
      WAITV(0); BAR();
#pragma unroll
      for (int i = 0; i < 4; ++i) st16<SC1>(rws, pkf[i], pk_dst + i * 40 * 1024);
    };
    auto mid = [&]() { WAITV(0); if (pk_flag >= 0 && lane == 0) set_flag(P.ws, pk_flag, tag); };
    f32x16 O;
    if (panel >= 2) attn_body<false>(lds, KB, VB, MISC, hh, qt, 0, lane, as_frag(q0), as_frag(q1), O, vwait, mid);
    else attn_body<true>(lds, KB, VB, MISC, hh, qt, 8 - 4 * panel - qt, lane, as_frag(q0), as_frag(q1), O, vwait, mid);
    const float inv = __builtin_amdgcn_rcpf(h ? O[8] : O[12]);
    const int t32 = 4 * panel + qt;
    u32x4 ov = {pk2(O[0] * inv, O[1] * inv), pk2(O[2] * inv, O[3] * inv), pk2(O[4] * inv, O[5] * inv), pk2(O[6] * inv, O[7] * inv)};
    st16<SC1>(rws, ov, OFF_OF + ((t32 * 40 + 2 * hp + hh) * 64 + lane) * 16);
    u32x2 ol = {pk2(O[8] * inv, O[9] * inv), pk2(O[10] * inv, O[11] * inv)};
    st8<SC1>(rws, ol, h ? OOB : OFF_OF + ((t32 * 40 + 32 + (hp >> 1)) * 64 + (hp & 1) * 32 + l31) * 16 + hh * 8);
    STAMP(6);
    WAITV(0);
    BAR();
    if (tid == 0) { set_flag(P.ws, OFF_F2 + (panel * 16 + hp) * 128, tag); set_flag(P.ws, OFF_FP + bid * 4, tag); }
    STAMP(7);
  }

  const int panel4 = 8 * (xcd >> 2) + (slot & 7), fg = 4 * (xcd & 3) + (slot >> 3);
  const int ntile = fg < 12 ? 7 : 6, tile = (fg < 12 ? 7 * fg : 84 + 6 * (fg - 12)) + (w < ntile ? w : 0);
  const int w4v = w < ntile ? lane * 16 : OOB, w4s = OFF_WGU + tile * 40 * 1024;
  u32x4 w4[40];
  {
    const int tq = w & 3, kh = w >> 2;
    const int t32 = 2 * tp3 + (tq >> 1), sub = tq & 1;
    const int token = 64 * tp3 + 16 * tq + l15;
    if (w < ntile) wait_flags(P.ws, OFF_FG + ((tile >> 2) * 64 + min(lane, 39)) * 4, tag);
    u32x4 wl[5];
#pragma unroll
    for (int nt = 0; nt < 5; ++nt) wl[nt] = ld16<SC1>(rws, kh ? lane * 16 : OOB, OFF_WO + ((5 * cg3 + nt) * 20 + 19) * 1024);
    u32x2 xr[3];
#pragma unroll
    for (int i = 0; i < 3; ++i) xr[i] = ld8<0>(rx, (kh && i == 2) ? OOB : token * 1280 + (80 * cg3 + 16 * (3 * kh + i) + 4 * g) * 2);
#pragma unroll
    for (int k = 0; k < 12; ++k) w4[k] = ld16<SC1>(rws, w4v, w4s + k * 1024);
    SB();
    const char* wb = lds + WOB + kh * 10240 + lane * 16;
    bf16x8 wf[4][5];
#pragma unroll
    for (int s = 0; s < 3; ++s)
#pragma unroll
      for (int nt = 0; nt < 5; ++nt) wf[s][nt] = lds_frag(wb + (nt * 19 + s) * 1024);
    SB();
    if (w == 7) wait_flags(P.ws, OFF_F2 + ((tp3 >> 1) * 16 + l15) * 128, tag);
    BAR();
    STAMP(8);
    const int ov = OFF_OF + ((t32 * 40 + 20 * kh + (g >> 1)) * 64 + (g & 1) * 32 + sub * 16 + l15) * 16;
    u32x4 of[10];
#pragma unroll
    for (int s = 0; s < 10; ++s) of[s] = ld16<SC1>(rws, ov, s * 2048);
    if (!kh) {
#pragma unroll
      for (int nt = 0; nt < 5; ++nt) wl[nt] = *(const LAS u32x4*)(wb + (nt * 19 + 9) * 1024);
    }
    SB();
    f32x4 acc[5] = {};
#pragma unroll
    for (int s = 0; s < 9; ++s) {
      if (s + 3 < 9) {
#pragma unroll
        for (int nt = 0; nt < 5; ++nt) wf[(s + 3) & 3][nt] = lds_frag(wb + (nt * 19 + s + 3) * 1024);
      }
#pragma unroll
      for (int nt = 0; nt < 5; ++nt) acc[nt] = MFMA16(wf[s & 3][nt], as_frag(of[s]), acc[nt]);
      SB();
    }
#pragma unroll
    for (int nt = 0; nt < 5; ++nt) acc[nt] = MFMA16(as_frag(wl[nt]), as_frag(of[9]), acc[nt]);
    STAMP(15);
    char* xb = lds + tq * 5120 + lane * 16;
    if (kh) { *(LAS f32x4*)(xb) = acc[0]; *(LAS f32x4*)(xb + 1024) = acc[1]; *(LAS f32x4*)(xb + 2048) = acc[2]; }
    else { *(LAS f32x4*)(xb + 3072) = acc[3]; *(LAS f32x4*)(xb + 4096) = acc[4]; }
    BAR();
    f32x4 r[3];
    if (kh) { r[0] = acc[3] + *(const LAS f32x4*)(xb + 3072); r[1] = acc[4] + *(const LAS f32x4*)(xb + 4096); r[2] = f32x4{}; }
    else { r[0] = acc[0] + *(const LAS f32x4*)(xb); r[1] = acc[1] + *(const LAS f32x4*)(xb + 1024); r[2] = acc[2] + *(const LAS f32x4*)(xb + 2048); }
    float sum = 0.f, sq = 0.f;
#pragma unroll
    for (int i = 0; i < 3; ++i) {
      const unsigned a0 = pk2(r[i][0], r[i][1]), a1 = pk2(r[i][2], r[i][3]);
      const u32x2 hv = {pk2(bflo(xr[i][0]) + bflo(a0), bfhi(xr[i][0]) + bfhi(a0)), pk2(bflo(xr[i][1]) + bflo(a1), bfhi(xr[i][1]) + bfhi(a1))};
      const int kap = 5 * cg3 + 3 * kh + i;
      st8<SC1>(rws, hv, (kh && i == 2) ? OOB : OFF_HF + ((t32 * 40 + kap) * 64 + (g >> 1) * 32 + sub * 16 + l15) * 16 + (g & 1) * 8);
      sum = dot2(hv[0], ONES2, sum); sum = dot2(hv[1], ONES2, sum);
      sq = dot2(hv[0], hv[0], sq); sq = dot2(hv[1], hv[1], sq);
    }
    sum += __shfl_xor(sum, 16); sq += __shfl_xor(sq, 16);
    sum += xhalf(sum); sq += xhalf(sq);
    st8<SC1>(rws, u32x2{__float_as_uint(sum), __float_as_uint(sq)}, lane < 16 ? OFF_HST + ((tp3 * 16 + cg3 * 2 + kh) * 64 + 16 * tq + l15) * 8 : OOB);
    STAMP(9);
#pragma unroll
    for (int k = 12; k < 18; ++k) w4[k] = ld16<SC1>(rws, w4v, w4s + k * 1024);
    SB();
    WAITV(6);
    BAR();
    if (tid == 448) set_flag(P.ws, OFF_F3 + (tp3 * 8 + cg3) * 128, tag);
    STAMP(10);
  }

  constexpr int WDA = 81920, STT = MISC + 64, CSX = MISC + 1088;
  const int panel5 = 2 * xcd + (slot & 1), cg5 = slot >> 1;
  const int wdsrc = OFF_WD + cg5 * 54 * 2560;
  {
    const int panel = panel4;
    const int hsrc = OFF_HF + 4 * panel * 40 * 1024 + w * 5 * 1024;
    auto hload = [&](int t) {
#pragma unroll
      for (int i = 0; i < 5; ++i) dma16<SC1>(rws, lds + (t & 1) * 40960 + (w * 5 + i) * 1024, lane * 16, hsrc + (t * 40 + i) * 1024);
    };
    if (w >= ntile) {
      if (w == 7) {
        wait_flags(P.ws, g == 1 ? OFF_F1 + (l15 * 16 + 15) * 128 : OFF_F3 + (2 * panel * 8 + l15) * 128, tag);
        STAMP(11);
      }
      BAR();
      if (w == 7) {
        u32x2 v[2][16];
#pragma unroll
        for (int hf = 0; hf < 2; ++hf)
#pragma unroll
          for (int i = 0; i < 16; ++i) v[hf][i] = ld8<SC1>(rws, OFF_HST + (((2 * panel + hf) * 16 + i) * 64 + lane) * 8);
        SB();
        hload(0);
#pragma unroll
        for (int hf = 0; hf < 2; ++hf) {
          float sum = 0.f, sq = 0.f;
#pragma unroll
          for (int i = 0; i < 16; ++i) { sum += __uint_as_float(v[hf][i][0]); sq += __uint_as_float(v[hf][i][1]); }
          float mean, rstd; ln_stats(sum, sq, mean, rstd);
          *(LAS f32x2*)(lds + STT + (64 * hf + lane) * 8) = f32x2{mean, rstd};
        }
      } else hload(0);
      WAITV(0);
      BAR();
#pragma unroll 1
      for (int t = 1; t < 4; ++t) {
        hload(t);
        if (w == 7) {
#pragma unroll 2
          for (int i = 0; i < 26; ++i) { const int pc = (t - 1) * 26 + i; dma16<SC1>(rws, lds + WDA + pc * 1024, pc * 1024 + lane * 16 < 31 * 2560 ? lane * 16 : OOB, wdsrc + pc * 1024); }
          WAITV(26);
        } else WAITV(0);
        BAR();
        STAMP(16 + t);
        if (t >= 2 && tid == 448) set_flag(P.ws, OFF_F4 + ((4 * panel + t - 2) * 16 + fg) * 128, tag);
      }
      if (w == 7) {
#pragma unroll 2
        for (int i = 0; i < 32; ++i) dma16<SC1>(rws, lds + i * 1024, lane * 16, wdsrc + 31 * 2560 + i * 1024);
      }
      BAR();
      if (tid == 448) set_flag(P.ws, OFF_F4 + ((4 * panel + 2) * 16 + fg) * 128, tag);
      WAITV(1);
      BAR();
      if (tid == 448) set_flag(P.ws, OFF_F4 + ((4 * panel + 3) * 16 + fg) * 128, tag);
    } else {
      const bool late = w >= 4;
#pragma unroll
      for (int k = 18; k < 40; ++k) w4[k] = ld16<SC1>(rws, lane * 16, w4s + k * 1024);
      SB();
      BAR();
      hload(0);
      float cs = 0.f;
#pragma unroll
      for (int k = 0; k < 40; ++k)
#pragma unroll
        for (int j = 0; j < 4; ++j) cs = dot2(w4[k][j], ONES2, cs);
      cs += xhalf(cs);
      if (lane < 32) *(LAS float*)(lds + CSX + w * 128 + l31 * 4) = cs;
      WAITL();
      f32x4 csr[4];
#pragma unroll
      for (int i = 0; i < 4; ++i) csr[i] = *(const LAS f32x4*)(lds + CSX + w * 128 + (8 * i + 4 * h) * 4);
      WAITV(0);
      f32x16 acc;
      auto epi = [&](int t) {
        const f32x2 st = *(const LAS f32x2*)(lds + STT + (32 * t + l31) * 8);
        const float nmean = -st.x, rs = st.y;
        constexpr float C0 = -2.0f * 0.7978845608028654f * 1.4426950408889634f, C1 = C0 * 0.044715f;
        const float r2 = smul(rs, rs);
        const float a0 = smul(C0, rs), a1 = smul(smul(C1, rs), r2), ir2 = __builtin_amdgcn_rcpf(r2);
        float a[8];
#pragma unroll
        for (int r = 0; r < 8; ++r) {
          const float G = sfma(nmean, csr[r >> 2][r & 3], acc[r]), U = sfma(nmean, csr[2 + (r >> 2)][r & 3], acc[r + 8]);
          const float ex = __builtin_amdgcn_exp2f(smul(G, sfma(smul(G, G), a1, a0)));
          a[r] = smul_t(smul(G, U), __builtin_amdgcn_rcpf(sfma_t(ex, ir2, ir2)));
        }
        st16<SC1>(rws, u32x4{pk2(a[0], a[1]), pk2(a[2], a[3]), pk2(a[4], a[5]), pk2(a[6], a[7])},
                  OFF_ACT + (((4 * panel + t) * 108 + tile) * 64 + lane) * 16);
      };
#pragma unroll 1
      for (int t = 0; t < 4; ++t) {
        BAR();
        STAMP(16 + t);
        if (late) { if (t > 0) epi(t - 1); if (t < 3) hload(t + 1); }
        const char* hb = lds + (t & 1) * 40960 + lane * 16;
        bf16x8 hf[8];
#pragma unroll
        for (int k = 0; k < 6; ++k) hf[k] = lds_frag(hb + k * 1024);
        acc = f32x16{};
        SB();
#pragma unroll
        for (int k = 0; k < 40; ++k) {
          if (k + 6 < 40) hf[(k + 6) & 7] = lds_frag(hb + (k + 6) * 1024);
          acc = MFMA32(as_frag(w4[k]), hf[k & 7], acc);
          SB();
        }
        asm volatile("s_nop 7\n\ts_nop 7" : "+v"(acc));
        if (!late) { if (t < 3) hload(t + 1); epi(t); WAITV(1); } else WAITV(0);
      }
      STAMP(20);
      BAR();
      if (late) {
        epi(3);
        WAITV(0);
        BAR();
      }
      STAMP(21);
    }
    STAMP(12);
  }

  {
    const int t32 = 4 * panel5 + (w >> 1), sub = w & 1;
    const int m = cg5 >> 1, odd = cg5 & 1;
#pragma unroll
    for (int i = 0; i < 4; ++i) {
      const int pc = 32 + w + 8 * i;
      if (pc < 58) dma16<SC1>(rws, lds + pc * 1024, pc * 1024 + lane * 16 < 23 * 2560 ? lane * 16 : OOB, wdsrc + 31 * 2560 + pc * 1024);
    }
    u32x2 hr[3];
    wait_flags(P.ws, OFF_F4 + (t32 * 16 + l15) * 128, tag);
    STAMP(13);
    const int av = OFF_ACT + ((t32 * 108 + (g >> 1)) * 64 + (g & 1) * 32 + sub * 16 + l15) * 16;
#define P5_AHEAD 10
    constexpr int AH5 = P5_AHEAD;
    u32x4 af[54];
#pragma unroll
    for (int i = 0; i < AH5; ++i) af[i] = ld16<SC1>(rws, av, i * 2048);
    SB();
    if (w < 4) BAR();
    f32x4 acc[3] = {};
    const int hoff = 2048 + g * 128 + (l15 & 7) * 16;
    bf16x8 wf[4][3];
    auto wread = [&](int s) {
      const char* wb = lds + (s < 31 ? WDA + s * 2560 : (s - 31) * 2560);
      wf[s & 3][0] = lds_frag(wb + lane * 16); wf[s & 3][1] = lds_frag(wb + 1024 + lane * 16); wf[s & 3][2] = lds_frag(wb + hoff);
    };
    wread(0); wread(1); wread(2);
#pragma unroll
    for (int s = 0; s < 54; ++s) {
      if (s == 40) BAR();
      if (s + 3 < 54) wread(s + 3);
      acc[0] = MFMA16(wf[s & 3][0], as_frag(af[s]), acc[0]);
      acc[1] = MFMA16(wf[s & 3][1], as_frag(af[s]), acc[1]);
      acc[2] = MFMA16(wf[s & 3][2], as_frag(af[s]), acc[2]);
      if (s + AH5 < 54) af[s + AH5] = ld16<SC1>(rws, av, (s + AH5) * 2048);
      if (s + AH5 == 54) {
#pragma unroll
        for (int i = 0; i < 3; ++i) {
          const int nt = i < 2 ? 5 * m + 3 * odd + i : 5 * m + 2;
          const int gg = i < 2 ? g : (2 * odd + (g & 1));
          hr[i] = ld8<SC1>(rws, OFF_HF + ((t32 * 40 + nt) * 64 + (gg >> 1) * 32 + sub * 16 + l15) * 16 + (gg & 1) * 8);
        }
      }
      SB();
    }
    const Buf ro = mk_rsrc(P.out, 2048 * 640 * 2);
    const int token = 32 * t32 + 16 * sub + l15;
#pragma unroll
    for (int i = 0; i < 3; ++i) {
      const unsigned y0 = pk2(acc[i][0], acc[i][1]), y1 = pk2(acc[i][2], acc[i][3]);
      const u32x2 ov = {pk2(bflo(hr[i][0]) + bflo(y0), bfhi(hr[i][0]) + bfhi(y0)), pk2(bflo(hr[i][1]) + bflo(y1), bfhi(hr[i][1]) + bfhi(y1))};
      const int col = i < 2 ? 16 * (5 * m + 3 * odd + i) + 4 * g : 16 * (5 * m + 2) + 8 * odd + 4 * g;
      st8<0>(ro, ov, (i == 2 && g >= 2) ? OOB : token * 1280 + col * 2);
    }
    if (bid == 0 && w == 0) {
      for (int spin = 0; spin < (1 << 17); ++spin) {
        u32x4 v = ld16<SC1>(rws, OFF_FP + lane * 16);
        asm volatile("" ::: "memory");
        if (__all(v[0] == tag && v[1] == tag && v[2] == tag && v[3] == tag)) break;
        __builtin_amdgcn_s_sleep(1);
      }
      if (lane == 0) set_flag(P.ws, OFF_EPOCH, tag);
    }
    STAMP(14);
    STAMP_FLUSH();
  }
}

extern "C" void kernel_launch(void* const* d_in, const int* in_sizes, int n_in, void* d_out, int out_size, void* d_ws,
                              size_t ws_size, hipStream_t stream) {
  (void)in_sizes; (void)n_in; (void)out_size; (void)ws_size;
  Params p;
  p.x = d_in[0]; p.wqkv = d_in[1]; p.wo = d_in[2]; p.wg = d_in[4]; p.wu = d_in[5]; p.wd = d_in[6];
  p.out = d_out; p.ws = d_ws;
  (void)hipFuncSetAttribute((const void*)fused_block, hipFuncAttributeMaxDynamicSharedMemorySize, LDS_BYTES);
  fused_block<<<256, 512, LDS_BYTES, stream>>>(p);
}
```

```cpp
#include <hip/hip_runtime.h>
#include <stdint.h>

typedef __bf16 bf16x8 __attribute__((ext_vector_type(8)));
typedef __bf16 bf16x4 __attribute__((ext_vector_type(4)));
typedef __bf16 bf16x2 __attribute__((ext_vector_type(2)));
typedef float f32x16 __attribute__((ext_vector_type(16)));
typedef float f32x4 __attribute__((ext_vector_type(4)));
typedef float f32x2 __attribute__((ext_vector_type(2)));
typedef unsigned u32x4 __attribute__((ext_vector_type(4)));
typedef unsigned u32x2 __attribute__((ext_vector_type(2)));
typedef unsigned long long u64;
#define LAS __attribute__((address_space(3)))
#define GAS __attribute__((address_space(1)))
#define DI __device__ __forceinline__
typedef __amdgpu_buffer_rsrc_t rsrc_t;
template <int N> struct IC { static constexpr int value = N; };

constexpr int OFF_EPOCH = 0;
constexpr int OFF_XST = 4096;
constexpr int OFF_FW = 20480;
constexpr int OFF_F1 = 32768;
constexpr int OFF_F4 = OFF_F1 + 256 * 128;
constexpr int OFF_FG = OFF_F4 + 1024 * 128;
constexpr int OFF_F2 = OFF_FG + 1088 * 128;
constexpr int OFF_F3 = OFF_F2 + 2048 * 128;
constexpr int OFF_HST = OFF_F3 + 2048 * 128;
constexpr int OFF_CSP = OFF_HST + 262144;
constexpr int OFF_QKV = OFF_CSP + 108 * 40 * 128;
constexpr int OFF_OF = OFF_QKV + 3 * 80 * 2048 * 16;
constexpr int OFF_HF = OFF_OF + 2048 * 640 * 2;
constexpr int OFF_ACT = OFF_HF + 2048 * 640 * 2;
constexpr int OFF_WGU = OFF_ACT + 2048 * 1728 * 2;
constexpr int OFF_WO = OFF_WGU + 108 * 40 * 1024;
constexpr int WS_END = OFF_WO + 40 * 20 * 1024;
constexpr int OFF_DBG = WS_END;
constexpr int WS_BYTES = WS_END + 256 * 512 * 4;
constexpr int LDS_BYTES = 163840;
#define CODE_SPAN 57344
constexpr int SC1 = 16;
constexpr int OOB = 0x70000000;

struct Params {
  const void* x; const void* wqkv; const void* wo; const void* wg; const void* wu; const void* wd;
  void* out; void* ws;
};

struct Buf { rsrc_t r; u32x4 q; };
DI Buf mk_rsrc(const void* p, int bytes) {
  Buf b; b.r = __builtin_amdgcn_make_buffer_rsrc((void*)p, 0, bytes, 0x00020000);
  const u64 a = (u64)p; b.q = u32x4{(unsigned)a, (unsigned)(a >> 32) & 0xffffu, (unsigned)bytes, 0x00020000u};
  return b;
}
DI unsigned lds_addr(const char* p) { return (unsigned)(uintptr_t)(const LAS char*)p; }
template <int AUX> DI void dma16(const Buf& b, char* lds_dst, int voff, int soff) {
  if constexpr (AUX == 0)
    asm volatile("s_mov_b32 m0, %0\n\ts_nop 0\n\tbuffer_load_dwordx4 %1, %2, %3 offen lds" ::"s"(lds_addr(lds_dst)), "v"(voff), "s"(b.q), "s"(soff) : "memory");
  else
    asm volatile("s_mov_b32 m0, %0\n\ts_nop 0\n\tbuffer_load_dwordx4 %1, %2, %3 offen sc1 lds" ::"s"(lds_addr(lds_dst)), "v"(voff), "s"(b.q), "s"(soff) : "memory");
}
DI void dma4(const Buf& b, char* lds_dst, int voff, int soff) {
  asm volatile("s_mov_b32 m0, %0\n\ts_nop 0\n\tbuffer_load_dword %1, %2, %3 offen lds" ::"s"(lds_addr(lds_dst)), "v"(voff), "s"(b.q), "s"(soff) : "memory");
}
template <int AUX> DI u32x4 hld16(const Buf& b, int voff, int soff = 0) {
  u32x4 r;
  if constexpr (AUX == 0) asm volatile("buffer_load_dwordx4 %0, %1, %2, %3 offen" : "=v"(r) : "v"(voff), "s"(b.q), "s"(soff) : "memory");
  else asm volatile("buffer_load_dwordx4 %0, %1, %2, %3 offen sc1" : "=v"(r) : "v"(voff), "s"(b.q), "s"(soff) : "memory");
  return r;
}
template <int AUX> DI u32x2 hld8(const Buf& b, int voff) {
  u32x2 r;
  if constexpr (AUX == 0) asm volatile("buffer_load_dwordx2 %0, %1, %2, 0 offen" : "=v"(r) : "v"(voff), "s"(b.q) : "memory");
  else asm volatile("buffer_load_dwordx2 %0, %1, %2, 0 offen sc1" : "=v"(r) : "v"(voff), "s"(b.q) : "memory");
  return r;
}
template <int AUX> DI unsigned hld4(const Buf& b, int voff) {
  unsigned r;
  if constexpr (AUX == 0) asm volatile("buffer_load_dword %0, %1, %2, 0 offen" : "=v"(r) : "v"(voff), "s"(b.q) : "memory");
  else asm volatile("buffer_load_dword %0, %1, %2, 0 offen sc1" : "=v"(r) : "v"(voff), "s"(b.q) : "memory");
  return r;
}
template <int AUX> DI u32x4 ld16(const Buf& rs, int voff, int soff = 0) { return __builtin_amdgcn_raw_buffer_load_b128(rs.r, voff, soff, AUX); }
template <int AUX> DI u32x2 ld8(const Buf& rs, int voff, int soff = 0) { return __builtin_amdgcn_raw_buffer_load_b64(rs.r, voff, soff, AUX); }
template <int AUX> DI void st16(const Buf& rs, u32x4 v, int voff) { __builtin_amdgcn_raw_buffer_store_b128(v, rs.r, voff, 0, AUX); }
template <int AUX> DI void st8(const Buf& rs, u32x2 v, int voff, int soff = 0) { __builtin_amdgcn_raw_buffer_store_b64(v, rs.r, voff, soff, AUX); }
DI bf16x8 as_frag(u32x4 v) { return __builtin_bit_cast(bf16x8, v); }
DI bf16x8 lds_frag(const char* p) { return *(const LAS bf16x8*)(p); }
DI bf16x4 lds_tr(const char* p) { return __builtin_amdgcn_ds_read_tr16_b64_v4bf16((LAS bf16x4*)(p)); }
DI bf16x8 cat(bf16x4 a, bf16x4 b) { return __builtin_shufflevector(a, b, 0, 1, 2, 3, 4, 5, 6, 7); }
DI unsigned pk2(float lo, float hi) { f32x2 v = {lo, hi}; return __builtin_bit_cast(unsigned, __builtin_convertvector(v, bf16x2)); }
DI float rbf(float v) { return __uint_as_float(pk2(v, v) << 16); }
DI float bflo(unsigned u) { return __uint_as_float(u << 16); }
DI float bfhi(unsigned u) { return __uint_as_float(u & 0xffff0000u); }
DI float dot2(unsigned a, unsigned b, float c) {
  return __builtin_amdgcn_fdot2_f32_bf16(__builtin_bit_cast(bf16x2, a), __builtin_bit_cast(bf16x2, b), c, false);
}
DI float sfma(float a, float b, float c) { float d; asm("v_fma_f32 %0, %1, %2, %3" : "=v"(d) : "v"(a), "v"(b), "v"(c)); return d; }
DI float sadd(float a, float b) { float d; asm("v_add_f32 %0, %1, %2" : "=v"(d) : "v"(a), "v"(b)); return d; }
DI float smul(float a, float b) { float d; asm("v_mul_f32 %0, %1, %2" : "=v"(d) : "v"(a), "v"(b)); return d; }
DI float sfma_t(float a, float b, float c) { float d; asm("s_nop 0\n\tv_fma_f32 %0, %1, %2, %3" : "=v"(d) : "v"(a), "v"(b), "v"(c)); return d; }
DI float smul_t(float a, float b) { float d; asm("s_nop 0\n\tv_mul_f32 %0, %1, %2" : "=v"(d) : "v"(a), "v"(b)); return d; }
#define MFMA32(a, b, c) __builtin_amdgcn_mfma_f32_32x32x16_bf16((a), (b), (c), 0, 0, 0)
#define MFMA16(a, b, c) __builtin_amdgcn_mfma_f32_16x16x32_bf16((a), (b), (c), 0, 0, 0)
#define WAITV(n) asm volatile("s_waitcnt vmcnt(" #n ")" ::: "memory")
#define WAITVN(n) asm volatile("s_waitcnt vmcnt(%0)" :: "n"(n) : "memory")
#define WAITL() asm volatile("s_waitcnt lgkmcnt(0)" ::: "memory")
#define SB() __builtin_amdgcn_sched_barrier(0)
#define BAR() do { asm volatile("s_waitcnt lgkmcnt(0)\n\ts_barrier" ::: "memory"); } while (0)
constexpr unsigned ONES2 = 0x3f803f80u;

DI float xhalf(float v) { return __shfl_xor(v, 32); }
DI void set_flag(void* ws, int off, unsigned tag) {
  __hip_atomic_store((GAS unsigned*)((GAS char*)ws + off), tag, __ATOMIC_RELAXED, __HIP_MEMORY_SCOPE_AGENT);
}
DI void set_line(const Buf& rs, int off, unsigned tag, int lane) { __builtin_amdgcn_raw_buffer_store_b32(tag, rs.r, lane < 32 ? off + lane * 4 : OOB, 0, SC1); }
DI void wait_flags(void* ws, int off, unsigned tag) {
  GAS unsigned* p = (GAS unsigned*)((GAS char*)ws + off);
#pragma unroll 1
  for (int spin = 0; spin < (1 << 17); ++spin) {
    unsigned v = __hip_atomic_load(p, __ATOMIC_RELAXED, __HIP_MEMORY_SCOPE_AGENT);
    if (__all(v == tag)) break;
    __builtin_amdgcn_s_sleep(1);
  }
  __builtin_amdgcn_fence(__ATOMIC_ACQUIRE, "wavefront");
}
template <int CTRL> DI float dpp_add(float v) { return v + __builtin_bit_cast(float, __builtin_amdgcn_update_dpp(0, __builtin_bit_cast(int, v), CTRL, 0xf, 0xf, true)); }
DI float wave_sum(float v) {
  v = dpp_add<0xB1>(v); v = dpp_add<0x4E>(v); v = dpp_add<0x141>(v); v = dpp_add<0x140>(v);
  v += __shfl_xor(v, 16); v += __shfl_xor(v, 32);
  return v;
}
DI void ln_stats(float sum, float sq, float& mean, float& rstd) {
  float m = sum * (1.0f / 640.0f);
  float var = fmaxf(sq * (1.0f / 640.0f) - m * m, 0.0f);
  mean = rbf(m);
  float s = rbf(__builtin_amdgcn_sqrtf(rbf(rbf(var) + 1e-5f)));
  rstd = __builtin_amdgcn_rcpf(s);
}

#define STAMP(i) do {} while (0)
#define STAMPC(i) do {} while (0)
#define STAMP_DECL do {} while (0)
#define STAMP_FLUSH() do {} while (0)

constexpr int CHS = 6144 + 64;
template <bool EDGE, class F, class G>
DI void attn_body(char* lds, int KB, int VB, int ONE, int hh, int qt, int i0, int lane, bf16x8 q0, bf16x8 q1, f32x16& O, F vwait, G mid) {
  const int l31 = lane & 31, h = lane >> 5;
  const char* kp0 = lds + KB + (32 * qt + l31) * 16 + ((hh ? 3 : 0) + h) * CHS;
  const char* kp1 = lds + KB + (32 * qt + l31) * 16 + 2 * CHS;
  constexpr float NEG = -1e30f;
  f32x16 S[8];
  bf16x8 k0[9], k1[9];
  constexpr int ord[9] = {8, 0, 1, 2, 3, 4, 5, 6, 7};
#pragma unroll
  for (int n = 0; n < 3; ++n) { k0[n] = lds_frag(kp0 + ord[n] * 512); k1[n] = lds_frag(kp1 + ord[n] * 512); }
  float m0 = NEG, m1 = NEG;
  f32x16 D;
#pragma unroll
  for (int n = 0; n < 9; ++n) {
    if (n + 3 < 9) { k0[n + 3] = lds_frag(kp0 + ord[n + 3] * 512); k1[n + 3] = lds_frag(kp1 + ord[n + 3] * 512); }
    f32x16 z = {};
    z = MFMA32(k0[n], q0, z);
    z = MFMA32(k1[n], q1, z);
    if (n == 0) D = z; else S[n - 1] = z;
    if (n >= 2) {
      const int i = n - 2;
      if (EDGE) {
#pragma unroll
        for (int r = 0; r < 16; ++r) S[i][r] = (i < i0) ? NEG : S[i][r];
      }
      if (i == 0) {
#pragma unroll
        for (int r = 0; r < 16; ++r) S[0][r] = ((r & 3) + 8 * (r >> 2) + 4 * h <= l31) ? D[r] : S[0][r];
      }
#pragma unroll
      for (int r = 0; r < 16; r += 4) { m0 = fmaxf(fmaxf(m0, S[i][r]), S[i][r + 1]); m1 = fmaxf(fmaxf(m1, S[i][r + 2]), S[i][r + 3]); }
      asm volatile("" : "+v"(m0), "+v"(m1));
    }
    SB();
  }
  if (EDGE) {
#pragma unroll
    for (int r = 0; r < 16; ++r) S[7][r] = (7 < i0) ? NEG : S[7][r];
  }
#pragma unroll
  for (int r = 0; r < 16; r += 4) { m0 = fmaxf(fmaxf(m0, S[7][r]), S[7][r + 1]); m1 = fmaxf(fmaxf(m1, S[7][r + 2]), S[7][r + 3]); }
  float m = fmaxf(m0, m1);
  m = fmaxf(m, xhalf(m));
  constexpr float C = 0.22360679774997896f * 1.4426950408889634f;
  const f32x2 C2 = {C, C}, mc2 = {-m * C, -m * C};
  const int gi = lane >> 4, dh = gi & 1, q = (lane & 15) >> 2, p = lane & 3;
  const bool one = dh && (p == 1 || p == 2);
  const int c4 = min(hh * 5 + dh * 4 + p, 9);
  const int vbase = one ? ONE : (VB + (32 * qt + 4 * h + q) * 16 + (c4 >> 1) * CHS + (c4 & 1) * 8);
  const int vstr = one ? 0 : 128;
  vwait();
  auto vfrag = [&](int i, int s) { return cat(lds_tr(lds + vbase + vstr * (4 * i + 2 * s)), lds_tr(lds + vbase + vstr * (4 * i + 2 * s + 1))); };
  O = f32x16{};
  bf16x8 vf[2][2];
  vf[1][0] = vfrag(1, 0); vf[1][1] = vfrag(1, 1);
  auto scale = [&](int i) {
#pragma unroll
    for (int r = 0; r < 16; r += 2) {
      f32x2 a = {S[i][r], S[i][r + 1]};
      a = __builtin_elementwise_fma(a, C2, mc2);
      S[i][r] = a.x; S[i][r + 1] = a.y;
    }
    asm volatile("" : "+v"(S[i]));
  };
  scale(1);
  SB();
#pragma unroll
  for (int n = 1; n <= 8; ++n) {
    const int i = n & 7;
    if (n == 6) mid();
    if (n < 8) { vf[(n + 1) & 1][0] = vfrag((n + 1) & 7, 0); vf[(n + 1) & 1][1] = vfrag((n + 1) & 7, 1); }
    else { vf[1][0] = vfrag(8, 0); vf[1][1] = vfrag(8, 1); }
#pragma unroll
    for (int r = 0; r < 16; ++r) S[i][r] = __builtin_amdgcn_exp2f(S[i][r]);
    asm volatile("" : "+v"(S[i]));
    SB();
    u32x4 pk[2];
#pragma unroll
    for (int j = 0; j < 8; ++j) pk[j >> 2][j & 3] = pk2(S[i][2 * j], S[i][2 * j + 1]);
    if (n < 8) scale((n + 1) & 7);
    asm volatile("" : "+v"(pk[0]), "+v"(pk[1]));
    SB();
#pragma unroll
    for (int s = 0; s < 2; ++s) {
      if (n < 8) O = MFMA32(vf[n & 1][s], as_frag(pk[s]), O);
      else {
        u32x4 pd, po;
#pragma unroll
        for (int j = 0; j < 4; ++j) {
          const int r = 8 * s + 2 * j, kk = (r & 3) + 8 * (r >> 2) + 4 * h;
          const unsigned mk = (kk <= l31 ? 0xffffu : 0u) | (kk + 1 <= l31 ? 0xffff0000u : 0u);
          pd[j] = pk[s][j] & mk; po[j] = pk[s][j] & ~mk;
        }
        O = MFMA32(vf[0][s], as_frag(po), O);
        O = MFMA32(vf[1][s], as_frag(pd), O);
      }
    }
    SB();
  }
}

#define SRC_T 7
constexpr int p1_wait(int t) {
  int n = 0;
  if (t == 0) return 4;
  if (t == 1) n += 1;
  for (int k = t + 1; k <= t + 2 && k < 10; ++k) n += 4;
  const int it_of_tile = t < 2 ? -1 : (t == 2 ? -1 : t - 3);
  if (6 > it_of_tile && 6 < t) n += 2;
  if (SRC_T > it_of_tile && SRC_T < t) n += 4;
  return n;
}
__global__ void __launch_bounds__(512, 2) fused_block(Params P) {
  extern __shared__ __attribute__((aligned(16))) char lds[];
  const int tid = threadIdx.x, lane = tid & 63;
  const int w = __builtin_amdgcn_readfirstlane(tid >> 6);
  const int bid = blockIdx.x, xcd = bid & 7, slot = bid >> 3;
  const int l31 = lane & 31, h = lane >> 5, l15 = lane & 15, g = lane >> 4;
  const int q4 = l15 >> 2, p4 = lane & 3, r8 = lane >> 3, c8 = (lane & 7) * 16;
  const Buf rws = mk_rsrc(P.ws, WS_BYTES);
  const Buf rx = mk_rsrc(P.x, 2048 * 640 * 2);
  u64 pc0; asm volatile("s_getpc_b64 %0" : "=s"(pc0));
  STAMP_DECL;
  STAMP(0);
  unsigned epoch;
  unsigned tag;
  const int panel1 = 4 * (xcd >> 1) + (slot & 3), ct = 8 * (xcd & 1) + (slot >> 2);
  int pk_flag = OOB;
  bool wo_ok = false;
  const int srow = panel1 * 128 + ct * 8 + w;
  u32x4 sx4; unsigned sx1;
  auto auxload = [&]() {
    epoch = hld4<SC1>(rws, lane == 0 ? OFF_EPOCH : lane < 3 ? ((slot * 8 + w) * 2 + lane - 1) * 65536 + 4096 : OOB);
    sx4 = hld16<0>(rx, srow * 1280 + lane * 20);
    sx1 = hld4<0>(rx, srow * 1280 + lane * 20 + 16);
    const Buf rc = mk_rsrc((const void*)(pc0 & ~127ull), CODE_SPAN);
    dma4(rc, lds + 163072, ((slot * 8 + w) * 256 + lane * 128) | (lane < 2 ? 0 : OOB), 0);
  };
  auto xstats = [&]() {
    float v[10] = {bflo(sx4[0]), bfhi(sx4[0]), bflo(sx4[1]), bfhi(sx4[1]), bflo(sx4[2]), bfhi(sx4[2]), bflo(sx4[3]), bfhi(sx4[3]), bflo(sx1), bfhi(sx1)};
    float sum = 0.f;
#pragma unroll
    for (int i = 0; i < 10; ++i) sum += v[i];
    const float m = wave_sum(sum) * (1.0f / 640.0f);
    float sq = 0.f;
#pragma unroll
    for (int i = 0; i < 10; ++i) sq += (v[i] - m) * (v[i] - m);
    const float var = wave_sum(sq) * (1.0f / 640.0f);
    const float sd = __builtin_amdgcn_sqrtf(rbf(rbf(var) + 1e-5f));
    tag = __builtin_amdgcn_readfirstlane(epoch * 1664525u + 1013904223u);
    st8<SC1>(rws, u32x2{pk2(m, sd), tag}, lane == 0 ? OFF_XST + srow * 8 : OOB);
  };

  constexpr int KB = 0, VB = 5 * CHS, WOB = 10 * CHS, MISC = 161792;
  static_assert(WOB + 95 * 1024 <= MISC, "LDS map");
  const int cg3 = slot >> 2;
  auto woload = [&]() {
    if (!wo_ok) wait_flags(P.ws, OFF_FW + l15 * 128, tag);
#pragma unroll 4
    for (int i = 0; i < 12; ++i) {
      const int pc = min(w + 8 * i, 94), nt = pc / 19, s = pc - nt * 19;
      dma16<SC1>(rws, lds + WOB + pc * 1024, lane * 16, OFF_WO + ((5 * cg3 + nt) * 20 + s) * 1024);
    }
  };

  if (__builtin_expect(ct < 15, 1)) {
    const int panel = panel1;
    const Buf rw = mk_rsrc(P.wqkv, 640 * 1920 * 2);
    constexpr int XS = 0, WSB = 65536, ST = 131072, CSP = ST + 2048, CSF = CSP + 2048, TCH = CSF + 1024;
    const int xv0 = (panel * 128 + 16 * w + r8) * 1280 + ((lane & 7) ^ (r8 >> 1)) * 16;
    const int xv1 = (panel * 128 + 16 * w + 8 + r8) * 1280 + ((lane & 7) ^ (4 + (r8 >> 1))) * 16;
    const int wv = (8 * w + g) * 3840 + ct * 256 + (((q4 ^ g) << 2) | p4) * 16;
#define ROTX ((3 * (slot & 3) + (slot >> 2) + 5 * (xcd >> 2)) % 10)
    const int rot = ROTX;
    auto issue1 = [&](int t, int i) {
      char* xs = lds + XS + (t & 3) * 16384 + w * 2048;
      char* ws_ = lds + WSB + (t & 3) * 16384 + w * 2048;
      const int kt = t + rot >= 10 ? t + rot - 10 : t + rot;
      if (i == 0) dma16<0>(rx, xs, xv0, kt * 128);
      if (i == 1) dma16<0>(rw, ws_, wv, kt * 245760);
      if (i == 2) dma16<0>(rx, xs + 1024, xv1, kt * 128);
      if (i == 3) dma16<0>(rw, ws_ + 1024, wv, kt * 245760 + 15360);
    };
    auto issue = [&](int t) { issue1(t, 0); issue1(t, 1); issue1(t, 2); issue1(t, 3); };
    constexpr int AH = 3;
    STAMP(29);
    issue(0); STAMP(30); __builtin_amdgcn_s_barrier();
    auxload(); __builtin_amdgcn_s_barrier();
    issue(1); STAMP(31);
    STAMP(23);
    const int tt = w & 3, nh = w >> 2, f = tt ^ (2 * nh);
    const int row = 32 * tt + l31;
    const int e = (2 * f) ^ h ^ ((row >> 1) & 7);
    const char* xa[4]; const char* wa[4][2];
#pragma unroll
    for (int s = 0; s < 4; ++s) {
      xa[s] = lds + XS + row * 128 + (((2 * s) ^ e) * 16);
#pragma unroll
      for (int j = 0; j < 2; ++j)
        wa[s][j] = lds + WSB + (16 * (s ^ f) + 8 * h + q4) * 256 + (((2 * nh + j) ^ q4) * 64) + 32 * (g & 1) + 8 * p4;
    }
    f32x16 acc[2] = {};
    float cs[2] = {0.f, 0.f};
    bf16x8 xf[2][4], wf[2][4][2];
    u32x2 xs; u32x4 src[4]; unsigned fw;
    const int u = (bid < 224 ? bid : 112 + (bid >> 1)) * 5 + w;
    const bool dopk = w < 5 && u < 1080;
    const int pcb = u % 27, pks = u / 27;
#pragma unroll
    for (int t = 0; t <= 10; ++t) {
      if (t < 10) {
        WAITVN(p1_wait(t < 10 ? t : 9));
        BAR();
        if (t == 0) STAMP(24);
      }
      if (t == 0) { asm volatile("" : "+v"(epoch), "+v"(sx4), "+v"(sx1)); xstats(); issue(2); }
      if (t == 6) {
        xs = hld8<SC1>(rws, OFF_XST + (panel * 128 + row) * 8);
        fw = hld4<SC1>(rws, OFF_FW + l15 * 128);
      }
      if (t == SRC_T) {
        const Buf rg = mk_rsrc(P.wg, 640 * 1728 * 2), ru = mk_rsrc(P.wu, 640 * 1728 * 2);
        const int v = dopk ? (16 * pks + r8) * 3456 + pcb * 128 + c8 : OOB;
        src[0] = hld16<0>(rg, v); src[1] = hld16<0>(rg, v, 8 * 3456); src[2] = hld16<0>(ru, v); src[3] = hld16<0>(ru, v, 8 * 3456);
      }
      SB();
      const int so = (t & 3) * 16384, b = t & 1;
#pragma unroll
      for (int s = 0; s < 4; ++s) {
        if (t > 0) {
          acc[0] = MFMA32(wf[b ^ 1][s][0], xf[b ^ 1][s], acc[0]);
          acc[1] = MFMA32(wf[b ^ 1][s][1], xf[b ^ 1][s], acc[1]);
        }
        if (t < 10) {
          xf[b][s] = lds_frag(xa[s] + so);
          wf[b][s][0] = cat(lds_tr(wa[s][0] + so), lds_tr(wa[s][0] + so + 1024));
          wf[b][s][1] = cat(lds_tr(wa[s][1] + so), lds_tr(wa[s][1] + so + 1024));
        }
        if (t > 0) {
          const unsigned a = __builtin_bit_cast(u32x4, wf[b ^ 1][3][0])[s], c = __builtin_bit_cast(u32x4, wf[b ^ 1][3][1])[s];
          cs[0] = sadd(sadd(cs[0], bflo(a)), bfhi(a)); cs[1] = sadd(sadd(cs[1], bflo(c)), bfhi(c));
          asm volatile("" : "+v"(cs[0]), "+v"(cs[1]));
        }
        SB();
        if (t + AH < 10) issue1(t + AH, s);
        SB();
      }
    }
    STAMP(1);
    cs[0] += xhalf(cs[0]); cs[1] += xhalf(cs[1]);
    if (lane < 32) {
      *(LAS float*)(lds + CSP + (tt * 128 + 64 * nh + l31) * 4) = cs[0];
      *(LAS float*)(lds + CSP + (tt * 128 + 64 * nh + 32 + l31) * 4) = cs[1];
    }
    BAR();
    if (tid < 128) {
      const LAS float* c = (const LAS float*)(lds + CSP) + tid;
      *(LAS float*)(lds + CSF + tid * 4) = (c[0] + c[128]) + (c[256] + c[384]);
    }
    asm volatile("" : "+v"(xs), "+v"(fw));
#pragma unroll 1
    for (int spin = 0; spin < (1 << 17) && !__all(xs[1] == tag); ++spin) {
      __builtin_amdgcn_s_sleep(1);
      asm volatile("buffer_load_dwordx2 %0, %1, %2, 0 offen sc1\n\ts_waitcnt vmcnt(0)" : "=&v"(xs) : "v"(OFF_XST + (panel * 128 + row) * 8), "s"(rws.q) : "memory");
    }
    const float mean = bflo(xs[0]), rstd = __builtin_amdgcn_rcpf(bfhi(xs[0]));
    BAR();
    {
      f32x4 c[8];
#pragma unroll
      for (int i = 0; i < 8; ++i) c[i] = *(const LAS f32x4*)(lds + CSF + (64 * nh + 8 * i + 4 * h) * 4);
      SB();
      STAMP(22);
      const int qdst = OFF_QKV + ((ct * 16 + 8 * nh + h) * 2048 + panel * 128 + row) * 16;
#pragma unroll
      for (int i = 0; i < 8; i += 2) {
        unsigned pk[2][2];
#pragma unroll
        for (int e2 = 0; e2 < 2; ++e2) {
          const int j = (i + e2) >> 2, gq = (i + e2) & 3;
          const f32x4 cc = c[i + e2];
          pk[e2][0] = pk2((acc[j][4 * gq + 0] - mean * cc[0]) * rstd, (acc[j][4 * gq + 1] - mean * cc[1]) * rstd);
          pk[e2][1] = pk2((acc[j][4 * gq + 2] - mean * cc[2]) * rstd, (acc[j][4 * gq + 3] - mean * cc[3]) * rstd);
        }
        auto r0 = __builtin_amdgcn_permlane32_swap(pk[0][0], pk[1][0], false, false);
        auto r1 = __builtin_amdgcn_permlane32_swap(pk[0][1], pk[1][1], false, false);
        st16<SC1>(rws, u32x4{r0[0], r1[0], r0[1], r1[1]}, qdst + i * 2048 * 16);
      }
    }
    SB();
    wo_ok = __all(fw == tag);
    u32x4 pkf[4]; float pkc[2]; int pk_dst, pk_cd;
    {
      char* sc = lds + w * 4096;
      asm volatile("s_waitcnt vmcnt(4)" : "+v"(src[0]), "+v"(src[1]), "+v"(src[2]), "+v"(src[3]) :: "memory");
#pragma unroll
      for (int i = 0; i < 4; ++i) *(LAS u32x4*)(sc + i * 1024 + lane * 16) = src[i];
      WAITL();
      const char* a = sc + (g & 1) * 2048 + (8 * h + q4) * 128 + 8 * p4;
#pragma unroll
      for (int i = 0; i < 4; ++i) pkf[i] = __builtin_bit_cast(u32x4, cat(lds_tr(a + 32 * i), lds_tr(a + 32 * i + 512)));
#pragma unroll
      for (int i = 0; i < 4; i += 2) {
        float a0 = 0.f, a1 = 0.f;
#pragma unroll
        for (int j = 0; j < 4; ++j) { a0 = dot2(pkf[i][j], ONES2, a0); a1 = dot2(pkf[i + 1][j], ONES2, a1); }
        auto r = __builtin_amdgcn_permlane32_swap(__float_as_uint(a0), __float_as_uint(a1), false, false);
        pkc[i >> 1] = __uint_as_float(r[0]) + __uint_as_float(r[1]);
      }
      pk_cd = dopk ? OFF_CSP + ((4 * pcb + h) * 40 + pks) * 128 + l31 * 4 : OOB;
      pk_dst = dopk ? OFF_WGU + (4 * pcb * 40 + pks) * 1024 + lane * 16 : OOB;
      pk_flag = dopk ? OFF_FG + (pcb * 40 + pks) * 128 : OOB;
    }
    WAITV(0);
    BAR();
    if (w == 0) set_line(rws, OFF_F1 + (panel * 16 + ct) * 128, tag, lane);
    STAMP(2);
#pragma unroll
    for (int i = 0; i < 4; ++i) st16<SC1>(rws, pkf[i], pk_dst + i * 40 * 1024);
    __builtin_amdgcn_raw_buffer_store_b32(__float_as_uint(pkc[0]), rws.r, pk_cd, 0, SC1);
    __builtin_amdgcn_raw_buffer_store_b32(__float_as_uint(pkc[1]), rws.r, pk_cd + 2 * 40 * 128, 0, SC1);
  } else {
    const Buf ro_ = mk_rsrc(P.wo, 640 * 640 * 2);
    const int rank = ((xcd >> 1) * 4 + (slot & 3)) * 8 + w;
    char* sc = lds + w * 8192;
    auxload();
    __builtin_amdgcn_s_barrier();
#pragma unroll
    for (int k = 0; k < 2; ++k) {
      const int u = rank + 128 * k, cb = u % 10, s = u / 10;
      char* d = sc + k * 4096;
      const int vo = u < 200 ? ((s < 16 ? 8 : 20) * (r8 >> 2) + (r8 & 3)) * 1280 + c8 : OOB;
      const int b0 = (s < 16 ? 40 * s : 160 * (s - 16) + 16) * 1280 + cb * 128, d1 = (s < 16 ? 4 : 40) * 1280, d2 = (s < 16 ? 20 : 80) * 1280;
      dma16<0>(ro_, d, vo, b0); dma16<0>(ro_, d + 1024, vo, b0 + d1); dma16<0>(ro_, d + 2048, vo, b0 + d2); dma16<0>(ro_, d + 3072, vo, b0 + d1 + d2);
    }
    asm volatile("s_waitcnt vmcnt(8)" : "+v"(epoch), "+v"(sx4), "+v"(sx1) :: "memory");
    xstats();
    STAMP(3);
    WAITV(1);
#pragma unroll
    for (int k = 0; k < 2; ++k) {
      const int u = rank + 128 * k, cb = u % 10, s = u / 10;
      const char* a = sc + k * 4096 + (8 * g + q4) * 128 + 8 * p4;
#pragma unroll
      for (int i = 0; i < 4; ++i) {
        bf16x8 fr = cat(lds_tr(a + 32 * i), lds_tr(a + 32 * i + 512));
        st16<SC1>(rws, __builtin_bit_cast(u32x4, fr), u < 200 ? OFF_WO + ((4 * cb + i) * 20 + s) * 1024 + lane * 16 : OOB);
      }
    }
    WAITV(0);
    BAR();
    if (w == 0) set_line(rws, OFF_FW + panel1 * 128, tag, lane);
    {
      const Buf rd_ = mk_rsrc(P.wd, 1728 * 640 * 2);
#pragma unroll 1
      for (int i = 0; i < 17; ++i) dma16<0>(rd_, sc, lane * 16 + (i * 128 + rank) * 1024, 0);
      WAITV(0);
      BAR();
    }
  }

  {
    const int panel = panel1, hp = ct;
    woload();
    if (w == 7) {
      const int ctA = (40 * hp) >> 7, ctB = (40 * hp + 39) >> 7;
      const int i = lane < 14 ? lane : 0;
      const int sec = i < 2 ? 0 : (i < 8 ? 1 : 2);
      const int rem = i < 2 ? i : (i < 8 ? i - 2 : i - 8);
      int pp = panel - (rem >> 1); pp = pp < 0 ? panel : pp;
      wait_flags(P.ws, OFF_F1 + (pp * 16 + sec * 5 + ((rem & 1) ? ctB : ctA)) * 128, tag);
    }
    if (tid == 0) { *(LAS u32x4*)(lds + MISC) = u32x4{0x00003f80u, 0u, 0u, 0u}; *(LAS u32x4*)(lds + MISC + 16) = u32x4{}; }
    BAR();
    STAMP(4);
    const int hh = w & 1, qt = w >> 1;
    const int tok0 = 128 * (panel - 2);
    const int qoff = OFF_QKV + (5 * hp * 2048 + 128 * panel + 32 * qt + l31) * 16;
    u32x4 q0 = hld16<SC1>(rws, qoff + ((hh ? 3 : 0) + h) * 32768);
    u32x4 q1 = hld16<SC1>(rws, h ? OOB : qoff + 2 * 32768);
#pragma unroll
    for (int i = 0; i < 8; ++i) {
      const int isv = i >> 2, pi = min(w + 8 * (i & 3), 29), ch = pi / 6, part = pi - 6 * ch;
      const int rel = tok0 + part * 64;
      dma16<SC1>(rws, lds + isv * VB + ch * CHS + part * 1024, rel < 0 ? OOB : lane * 16, OFF_QKV + (((1 + isv) * 80 + 5 * hp + ch) * 2048 + (rel < 0 ? 0 : rel)) * 16);
      if (i == 3) __builtin_amdgcn_s_barrier();
    }
    asm volatile("s_waitcnt vmcnt(4)" : "+v"(q0), "+v"(q1) :: "memory");
    if (hh) { q1[0] = 0; q1[1] = 0; } else { q1[2] = 0; q1[3] = 0; }
    BAR();
    STAMP(5);
    set_line(rws, pk_flag, tag, lane);
    if (w < 4) __builtin_amdgcn_s_setprio(2);
    else { WAITV(0); BAR(); }
    STAMP(25);
    auto vwait = [&]() {
      STAMP(26);
      if (w < 4) { WAITV(0); BAR(); }
      STAMP(27);
    };
    auto mid = [&]() { STAMP(28); };
    f32x16 O;
    if (panel >= 2) attn_body<false>(lds, KB, VB, MISC, hh, qt, 0, lane, as_frag(q0), as_frag(q1), O, vwait, mid);
    else attn_body<true>(lds, KB, VB, MISC, hh, qt, 8 - 4 * panel - qt, lane, as_frag(q0), as_frag(q1), O, vwait, mid);
    const float inv = __builtin_amdgcn_rcpf(h ? O[8] : O[12]);
    const int t32 = 4 * panel + qt;
    u32x4 ov = {pk2(O[0] * inv, O[1] * inv), pk2(O[2] * inv, O[3] * inv), pk2(O[4] * inv, O[5] * inv), pk2(O[6] * inv, O[7] * inv)};
    st16<SC1>(rws, ov, OFF_OF + ((t32 * 40 + 2 * hp + hh) * 64 + lane) * 16);
    u32x2 ol = {pk2(O[8] * inv, O[9] * inv), pk2(O[10] * inv, O[11] * inv)};
    st8<SC1>(rws, ol, h ? OOB : OFF_OF + (t32 * 40 + 32) * 1024 + ((2 * hp + hh) * 32 + l31) * 8);
    STAMP(6);
    WAITV(0);
    set_line(rws, OFF_F2 + (t32 * 32 + 2 * hp + hh) * 128, tag, lane);
    STAMP(7);
  }

  const int panel4 = 8 * (xcd >> 2) + (slot & 7), fg = 4 * (xcd & 3) + (slot >> 3);
  const int ntile = fg < 12 ? 7 : 6, tile = (fg < 12 ? 7 * fg : 84 + 6 * (fg - 12)) + (w < ntile ? w : 0);
  const int w4v = w < ntile ? lane * 16 : OOB, w4s = OFF_WGU + tile * 40 * 1024;
  u32x4 cs5[5];
  u32x4 w4[40];
  {
    const int sub = w & 1, ng = (w >> 1) & 1;
    const int t32 = 8 * xcd + (slot & 1) + 4 * ((slot >> 1) & 1) + 2 * (w >> 2);
    const int token = 32 * t32 + 16 * sub + l15;
    u32x4 wl[3];
    u32x2 xr[3];
#pragma unroll
    for (int i = 0; i < 3; ++i) {
      wl[i] = ld16<SC1>(rws, (ng && i == 2) ? OOB : lane * 16, OFF_WO + ((5 * cg3 + 3 * ng + i) * 20 + 19) * 1024);
      xr[i] = ld8<0>(rx, (ng && i == 2) ? OOB : token * 1280 + (80 * cg3 + 16 * (3 * ng + i) + 4 * g) * 2);
    }
    if (w < ntile) wait_flags(P.ws, OFF_FG + ((tile >> 2) * 40 + min(lane, 39)) * 128, tag);
#pragma unroll
    for (int k = 0; k < 12; ++k) w4[k] = ld16<SC1>(rws, w4v, w4s + k * 1024);
    SB();
    const char* wb = lds + WOB + ng * 3 * 19 * 1024 + lane * 16;
    const int t2 = ng ? 19 * 1024 : 2 * 19 * 1024;
    bf16x8 wf[4][3];
    auto wread = [&](int s) { wf[s & 3][0] = lds_frag(wb + s * 1024); wf[s & 3][1] = lds_frag(wb + (19 + s) * 1024); wf[s & 3][2] = lds_frag(wb + t2 + s * 1024); };
    wread(0); wread(1); wread(2);
    SB();
    wait_flags(P.ws, OFF_F2 + (t32 * 32 + l31) * 128, tag);
    STAMP(8);
    const int ov = OFF_OF + ((t32 * 40 + (g >> 1)) * 64 + (g & 1) * 32 + sub * 16 + l15) * 16;
    u32x4 of[20];
#pragma unroll
    for (int s = 0; s < 16; ++s) of[s] = ld16<SC1>(rws, ov, s * 2048);
    const int ol = OFF_OF + (t32 * 40 + 32) * 1024 + (2 * g * 32 + sub * 16 + l15) * 8;
#pragma unroll
    for (int s = 16; s < 20; ++s) {
      const u32x2 a = ld8<SC1>(rws, ol, (s - 16) * 2048), b = ld8<SC1>(rws, ol + 256, (s - 16) * 2048);
      of[s] = u32x4{a[0], a[1], b[0], b[1]};
    }
    SB();
    f32x4 acc[3] = {};
#pragma unroll
    for (int s = 0; s < 19; ++s) {
      if (s + 3 < 19) wread(s + 3);
      acc[0] = MFMA16(wf[s & 3][0], as_frag(of[s]), acc[0]);
      acc[1] = MFMA16(wf[s & 3][1], as_frag(of[s]), acc[1]);
      if (!ng) acc[2] = MFMA16(wf[s & 3][2], as_frag(of[s]), acc[2]);
      SB();
    }
#pragma unroll
    for (int i = 0; i < 3; ++i) acc[i] = MFMA16(as_frag(wl[i]), as_frag(of[19]), acc[i]);
    STAMP(15);
    float sum = 0.f, sq = 0.f;
#pragma unroll
    for (int i = 0; i < 3; ++i) {
      const unsigned a0 = pk2(acc[i][0], acc[i][1]), a1 = pk2(acc[i][2], acc[i][3]);
      const u32x2 hv = {pk2(bflo(xr[i][0]) + bflo(a0), bfhi(xr[i][0]) + bfhi(a0)), pk2(bflo(xr[i][1]) + bflo(a1), bfhi(xr[i][1]) + bfhi(a1))};
      const int kap = 5 * cg3 + 3 * ng + i;
      st8<SC1>(rws, hv, (ng && i == 2) ? OOB : OFF_HF + ((t32 * 40 + kap) * 64 + (g >> 1) * 32 + sub * 16 + l15) * 16 + (g & 1) * 8);
      sum = dot2(hv[0], ONES2, sum); sum = dot2(hv[1], ONES2, sum);
      sq = dot2(hv[0], hv[0], sq); sq = dot2(hv[1], hv[1], sq);
    }
    sum += __shfl_xor(sum, 16); sq += __shfl_xor(sq, 16);
    sum += xhalf(sum); sq += xhalf(sq);
    st8<SC1>(rws, u32x2{__float_as_uint(sum), __float_as_uint(sq)}, lane < 16 ? OFF_HST + ((t32 * 16 + cg3 * 2 + ng) * 32 + 16 * sub + l15) * 8 : OOB);
    STAMP(9);
#pragma unroll
    for (int k = 12; k < 20; ++k) w4[k] = ld16<SC1>(rws, w4v, w4s + k * 1024);
#pragma unroll
    for (int i = 0; i < 5; ++i) cs5[i] = ld16<SC1>(rws, w4v, OFF_CSP + tile * 5120 + i * 1024);
    SB();
    WAITV(13);
    set_line(rws, OFF_F3 + (t32 * 32 + cg3 * 4 + (w & 3)) * 128, tag, lane);
    __builtin_amdgcn_s_setprio(0);
    STAMP(10);
  }

  constexpr int WDA = 81920, STT = MISC + 64, CSX = MISC + 1088;
  const int panel5 = 2 * xcd + (slot & 1), cg5 = slot >> 1;
  const Buf rwd = mk_rsrc(P.wd, 1728 * 640 * 2);
  auto wdpiece = [&](int part, int pc) {
    const int c = pc * 64 + lane, row = (c * 52429) >> 18, nrow = part ? 736 : 992;
    dma16<0>(rwd, lds + (part ? 0 : WDA) + pc * 1024, row < nrow ? row * 1200 + c * 16 + cg5 * 80 : OOB, part ? 992 * 1280 : 0);
  };
  {
    const int panel = panel4;
    const int hsrc = OFF_HF + 4 * panel * 40 * 1024;
    auto hload = [&](int t) {
      const int so = t < 2 ? ((w >> 2) * 40 + 20 * t + (w & 3) * 5) * 1024 : (t * 40 + w * 5) * 1024;
#pragma unroll
      for (int i = 0; i < 5; ++i) dma16<SC1>(rws, lds + (t & 1) * 40960 + (w * 5 + i) * 1024, lane * 16, hsrc + so + i * 1024);
    };
    auto wdload = [&](int t) {
      if (t == 1) {
#pragma unroll
        for (int i = 0; i < 10; ++i) wdpiece(0, (w - 4) * 10 + i);
      } else if (t == 2) {
#pragma unroll
        for (int i = 0; i < 5; ++i) wdpiece(0, min(40 + w * 5 + i, 77));
      } else {
#pragma unroll
        for (int i = 0; i < 5; ++i) wdpiece(1, w * 5 + i);
      }
    };
    if (w >= ntile) {
      auto stats = [&](int pr) {
        u32x2 v[16];
#pragma unroll
        for (int i = 0; i < 16; ++i) v[i] = ld8<SC1>(rws, OFF_HST + (((4 * panel + 2 * pr + h) * 16 + i) * 32 + l31) * 8);
        float sum = 0.f, sq = 0.f;
#pragma unroll
        for (int i = 0; i < 16; ++i) { sum += __uint_as_float(v[i][0]); sq += __uint_as_float(v[i][1]); }
        float mean, rstd; ln_stats(sum, sq, mean, rstd);
        *(LAS f32x2*)(lds + STT + (64 * pr + lane) * 8) = f32x2{mean, rstd};
      };
      if (w == 7) { wait_flags(P.ws, OFF_F3 + (4 * panel * 32 + lane) * 128, tag); STAMP(11); }
      BAR();
      hload(0);
      WAITV(0);
      BAR();
      hload(1);
      if (w == 7) stats(0);
      WAITV(0);
      BAR(); STAMP(17);
      if (w == 7) {
        wait_flags(P.ws, OFF_F3 + ((4 * panel + 2) * 32 + lane) * 128, tag);
        if (lane == 0) *(volatile LAS unsigned*)(lds + MISC + 16) = 1u;
        stats(1);
      }
      wdload(1);
      BAR(); STAMP(18);
      hload(3); wdload(2);
      WAITV(0);
      BAR(); STAMP(19);
      if (w == 7) { set_line(rws, OFF_F4 + (4 * panel * 16 + fg) * 128, tag, lane); set_line(rws, OFF_F4 + ((4 * panel + 1) * 16 + fg) * 128, tag, lane); }
      wdload(3);
      BAR();
      if (w == 7) set_line(rws, OFF_F4 + ((4 * panel + 2) * 16 + fg) * 128, tag, lane);
      WAITV(0);
      BAR();
      if (w == 7) set_line(rws, OFF_F4 + ((4 * panel + 3) * 16 + fg) * 128, tag, lane);
    } else {
      const bool late = w >= 4;
      f32x4 csr[4];
      {
        f32x4 c = __builtin_bit_cast(f32x4, cs5[0]);
#pragma unroll
        for (int i = 1; i < 5; ++i) c += __builtin_bit_cast(f32x4, cs5[i]);
#pragma unroll
        for (int j = 0; j < 4; ++j) { c[j] += __shfl_xor(c[j], 8); c[j] += __shfl_xor(c[j], 16); c[j] += xhalf(c[j]); }
        if (lane < 8) *(LAS f32x4*)(lds + CSX + w * 128 + lane * 16) = c;
        WAITL();
#pragma unroll
        for (int i = 0; i < 4; ++i) csr[i] = *(const LAS f32x4*)(lds + CSX + w * 128 + (8 * i + 4 * h) * 4);
      }
      BAR();
      hload(0);
      WAITV(0);
      f32x16 acc[2];
      auto epi = [&](int t, const f32x16& ac) {
        const f32x2 st = *(const LAS f32x2*)(lds + STT + (32 * t + l31) * 8);
        const float nmean = -st.x, rs = st.y;
        constexpr float C0 = -2.0f * 0.7978845608028654f * 1.4426950408889634f, C1 = C0 * 0.044715f;
        const float r2 = smul(rs, rs);
        const float a0 = smul(C0, rs), a1 = smul(smul(C1, rs), r2), ir2 = __builtin_amdgcn_rcpf(r2);
        float a[8];
#pragma unroll
        for (int r = 0; r < 8; ++r) {
          const float G = sfma(nmean, csr[r >> 2][r & 3], ac[r]), U = sfma(nmean, csr[2 + (r >> 2)][r & 3], ac[r + 8]);
          const float ex = __builtin_amdgcn_exp2f(smul(G, sfma(smul(G, G), a1, a0)));
          a[r] = smul_t(smul(G, U), __builtin_amdgcn_rcpf(sfma_t(ex, ir2, ir2)));
        }
        st16<SC1>(rws, u32x4{pk2(a[0], a[1]), pk2(a[2], a[3]), pk2(a[4], a[5]), pk2(a[6], a[7])},
                  OFF_ACT + (((4 * panel + t) * 108 + tile) * 64 + lane) * 16);
      };
      auto pass = [&](auto tc) {
        constexpr int t = decltype(tc)::value;
        const char* hb = lds + (t & 1) * 40960 + lane * 16;
        bf16x8 hf[8];
#pragma unroll
        for (int k = 0; k < 6; ++k) hf[k] = lds_frag(hb + k * 1024);
        if (t == 0) { acc[0] = f32x16{}; acc[1] = f32x16{}; }
        if (t >= 2) acc[t & 1] = f32x16{};
        SB();
#pragma unroll
        for (int k = 0; k < 40; ++k) {
          if (k + 6 < 40) hf[(k + 6) & 7] = lds_frag(hb + (k + 6) * 1024);
          if (t < 2) acc[k / 20] = MFMA32(as_frag(w4[20 * t + k % 20]), hf[k & 7], acc[k / 20]);
          else acc[t & 1] = MFMA32(as_frag(w4[k]), hf[k & 7], acc[t & 1]);
          if (t == 0 && (k & 1)) w4[20 + (k >> 1)] = ld16<SC1>(rws, lane * 16, w4s + (20 + (k >> 1)) * 1024);
          SB();
        }
        asm volatile("s_nop 7\n\ts_nop 7" : "+v"(acc[0]), "+v"(acc[1]));
      };
      BAR(); STAMP(16);
      hload(1);
      pass(IC<0>{});
      WAITV(20);
      BAR(); STAMP(17);
      if (late) wdload(1);
      pass(IC<1>{});
      if (!late) {
#pragma unroll 1
        for (int spin = 0; spin < (1 << 22) && *(volatile LAS unsigned*)(lds + MISC + 16) == 0u; ++spin) __builtin_amdgcn_s_sleep(1);
        asm volatile("" ::: "memory");
#pragma unroll
        for (int i = 0; i < 10; ++i) dma16<SC1>(rws, lds + (w * 10 + i) * 1024, lane * 16, hsrc + (80 + w * 10 + i) * 1024);
        epi(0, acc[0]); epi(1, acc[1]); WAITV(2);
      } else WAITV(0);
      BAR(); STAMP(18);
      if (late) { hload(3); epi(0, acc[0]); epi(1, acc[1]); }
      pass(IC<2>{});
      if (!late) { hload(3); wdload(2); epi(2, acc[0]); WAITV(1); } else { wdload(2); WAITV(5); }
      BAR(); STAMP(19);
      if (late) { wdload(3); epi(2, acc[0]); }
      pass(IC<3>{});
      if (!late) { wdload(3); epi(3, acc[1]); WAITV(1); } else WAITV(0);
      STAMP(20);
      BAR();
      if (late) {
        epi(3, acc[1]);
        WAITV(0);
        BAR();
      }
      STAMP(21);
    }
    STAMP(12);
  }

  {
    const int t32 = 4 * panel5 + (w >> 1), sub = w & 1;
#pragma unroll
    for (int i = 0; i < 3; ++i) wdpiece(1, min(40 + w + 8 * i, 57));
    u32x2 hr[3];
    wait_flags(P.ws, OFF_F4 + (t32 * 16 + l15) * 128, tag);
    STAMP(13);
    const int av = OFF_ACT + ((t32 * 108 + (g >> 1)) * 64 + (g & 1) * 32 + sub * 16 + l15) * 16;
#define P5_AHEAD 10
    constexpr int AH5 = P5_AHEAD;
    u32x4 af[54];
#pragma unroll
    for (int i = 0; i < AH5; ++i) af[i] = ld16<SC1>(rws, av, i * 2048);
    SB();
    if (w < 4) BAR();
    f32x4 acc[3] = {};
    const int woff = (16 * (g >> 1) + 4 * (g & 1) + q4) * 80 + 8 * p4;
    bf16x8 wf[4][3];
    auto wread = [&](int s) {
      const char* wb = lds + (s < 31 ? WDA + s * 2560 : (s - 31) * 2560) + woff;
#pragma unroll
      for (int i = 0; i < 3; ++i) wf[s & 3][i] = cat(lds_tr(wb + 32 * i), lds_tr(wb + 32 * i + 640));
    };
    wread(0); wread(1); wread(2);
#pragma unroll
    for (int s = 0; s < 54; ++s) {
      if (s == 44) BAR();
      if (s + 3 < 54) wread(s + 3);
      acc[0] = MFMA16(wf[s & 3][0], as_frag(af[s]), acc[0]);
      acc[1] = MFMA16(wf[s & 3][1], as_frag(af[s]), acc[1]);
      acc[2] = MFMA16(wf[s & 3][2], as_frag(af[s]), acc[2]);
      if (s + AH5 < 54) af[s + AH5] = ld16<SC1>(rws, av, (s + AH5) * 2048);
      if (s + AH5 == 54) {
#pragma unroll
        for (int i = 0; i < 3; ++i) {
          const int c4 = 10 * cg5 + 4 * i + g;
          hr[i] = ld8<SC1>(rws, (i == 2 && g >= 2) ? OOB : OFF_HF + ((t32 * 40 + (c4 >> 2)) * 64 + ((c4 >> 1) & 1) * 32 + sub * 16 + l15) * 16 + (c4 & 1) * 8);
        }
      }
      SB();
    }
    const Buf ro = mk_rsrc(P.out, 2048 * 640 * 2);
    const int token = 32 * t32 + 16 * sub + l15;
#pragma unroll
    for (int i = 0; i < 3; ++i) {
      const unsigned y0 = pk2(acc[i][0], acc[i][1]), y1 = pk2(acc[i][2], acc[i][3]);
      const u32x2 ov = {pk2(bflo(hr[i][0]) + bflo(y0), bfhi(hr[i][0]) + bfhi(y0)), pk2(bflo(hr[i][1]) + bflo(y1), bfhi(hr[i][1]) + bfhi(y1))};
      const int col = 40 * cg5 + 16 * i + 4 * g;
      st8<0>(ro, ov, (i == 2 && g >= 2) ? OOB : token * 1280 + col * 2);
    }
    if (bid == 0 && w == 0) {
#pragma unroll 1
      for (int spin = 0; spin < (1 << 17); ++spin) {
        bool ok = true;
#pragma unroll
        for (int i = 0; i < 16; ++i) { const u32x4 v = ld16<SC1>(rws, OFF_XST + lane * 16, i * 1024); ok = ok && v[1] == tag && v[3] == tag; }
        asm volatile("" ::: "memory");
        if (__all(ok)) break;
        __builtin_amdgcn_s_sleep(1);
      }
      if (lane == 0) set_flag(P.ws, OFF_EPOCH, tag);
    }
    STAMP(14);
    STAMP_FLUSH();
  }
}

extern "C" void kernel_launch(void* const* d_in, const int* in_sizes, int n_in, void* d_out, int out_size, void* d_ws,
                              size_t ws_size, hipStream_t stream) {
  (void)in_sizes; (void)n_in; (void)out_size; (void)ws_size;
  Params p;
  p.x = d_in[0]; p.wqkv = d_in[1]; p.wo = d_in[2]; p.wg = d_in[4]; p.wu = d_in[5]; p.wd = d_in[6];
  p.out = d_out; p.ws = d_ws;
  (void)hipFuncSetAttribute((const void*)fused_block, hipFuncAttributeMaxDynamicSharedMemorySize, LDS_BYTES);
  fused_block<<<256, 512, LDS_BYTES, stream>>>(p);
}
```

```cpp
#include <hip/hip_runtime.h>
#include <stdint.h>

typedef __bf16 bf16x8 __attribute__((ext_vector_type(8)));
typedef __bf16 bf16x4 __attribute__((ext_vector_type(4)));
typedef __bf16 bf16x2 __attribute__((ext_vector_type(2)));
typedef float f32x16 __attribute__((ext_vector_type(16)));
typedef float f32x4 __attribute__((ext_vector_type(4)));
typedef float f32x2 __attribute__((ext_vector_type(2)));
typedef unsigned u32x4 __attribute__((ext_vector_type(4)));
typedef unsigned u32x2 __attribute__((ext_vector_type(2)));
typedef unsigned long long u64;
#define LAS __attribute__((address_space(3)))
#define GAS __attribute__((address_space(1)))
#define DI __device__ __forceinline__
typedef __amdgpu_buffer_rsrc_t rsrc_t;
template <int N> struct IC { static constexpr int value = N; };

constexpr int OFF_EPOCH = 0;
constexpr int OFF_XST = 4096;
constexpr int OFF_FW = 20480;
constexpr int OFF_F1 = 32768;
constexpr int OFF_F4 = OFF_F1 + 256 * 128;
constexpr int OFF_FG = OFF_F4 + 1024 * 128;
constexpr int OFF_FD = OFF_FG + 1088 * 128;
constexpr int OFF_F2 = OFF_FD + 544 * 128;
constexpr int OFF_F3 = OFF_F2 + 2048 * 128;
constexpr int OFF_HST = OFF_F3 + 2048 * 128;
constexpr int OFF_CSP = OFF_HST + 262144;
constexpr int OFF_QKV = OFF_CSP + 108 * 40 * 128;
constexpr int OFF_OF = OFF_QKV + 3 * 80 * 2048 * 16;
constexpr int OFF_HF = OFF_OF + 2048 * 640 * 2;
constexpr int OFF_ACT = OFF_HF + 2048 * 640 * 2;
constexpr int OFF_WGU = OFF_ACT + 2048 * 1728 * 2;
constexpr int OFF_WO = OFF_WGU + 108 * 40 * 1024;
constexpr int OFF_WD = OFF_WO + 40 * 20 * 1024;
constexpr int WS_END = OFF_WD + 16 * 54 * 2560;
constexpr int OFF_DBG = WS_END;
constexpr int WS_BYTES = WS_END + 256 * 512 * 4;
constexpr int LDS_BYTES = 163840;
#define CODE_SPAN 57344
constexpr int SC1 = 16;
constexpr int OOB = 0x70000000;

struct Params {
  const void* x; const void* wqkv; const void* wo; const void* wg; const void* wu; const void* wd;
  void* out; void* ws;
};

struct Buf { rsrc_t r; u32x4 q; };
DI Buf mk_rsrc(const void* p, int bytes) {
  Buf b; b.r = __builtin_amdgcn_make_buffer_rsrc((void*)p, 0, bytes, 0x00020000);
  const u64 a = (u64)p; b.q = u32x4{(unsigned)a, (unsigned)(a >> 32) & 0xffffu, (unsigned)bytes, 0x00020000u};
  return b;
}
DI unsigned lds_addr(const char* p) { return (unsigned)(uintptr_t)(const LAS char*)p; }
template <int AUX> DI void dma16(const Buf& b, char* lds_dst, int voff, int soff) {
  if constexpr (AUX == 0)
    asm volatile("s_mov_b32 m0, %0\n\ts_nop 0\n\tbuffer_load_dwordx4 %1, %2, %3 offen lds" ::"s"(lds_addr(lds_dst)), "v"(voff), "s"(b.q), "s"(soff) : "memory");
  else
    asm volatile("s_mov_b32 m0, %0\n\ts_nop 0\n\tbuffer_load_dwordx4 %1, %2, %3 offen sc1 lds" ::"s"(lds_addr(lds_dst)), "v"(voff), "s"(b.q), "s"(soff) : "memory");
}
DI void dma4(const Buf& b, char* lds_dst, int voff, int soff) {
  asm volatile("s_mov_b32 m0, %0\n\ts_nop 0\n\tbuffer_load_dword %1, %2, %3 offen lds" ::"s"(lds_addr(lds_dst)), "v"(voff), "s"(b.q), "s"(soff) : "memory");
}
template <int AUX> DI u32x4 hld16(const Buf& b, int voff, int soff = 0) {
  u32x4 r;
  if constexpr (AUX == 0) asm volatile("buffer_load_dwordx4 %0, %1, %2, %3 offen" : "=v"(r) : "v"(voff), "s"(b.q), "s"(soff) : "memory");
  else asm volatile("buffer_load_dwordx4 %0, %1, %2, %3 offen sc1" : "=v"(r) : "v"(voff), "s"(b.q), "s"(soff) : "memory");
  return r;
}
template <int AUX> DI u32x2 hld8(const Buf& b, int voff) {
  u32x2 r;
  if constexpr (AUX == 0) asm volatile("buffer_load_dwordx2 %0, %1, %2, 0 offen" : "=v"(r) : "v"(voff), "s"(b.q) : "memory");
  else asm volatile("buffer_load_dwordx2 %0, %1, %2, 0 offen sc1" : "=v"(r) : "v"(voff), "s"(b.q) : "memory");
  return r;
}
template <int AUX> DI unsigned hld4(const Buf& b, int voff) {
  unsigned r;
  if constexpr (AUX == 0) asm volatile("buffer_load_dword %0, %1, %2, 0 offen" : "=v"(r) : "v"(voff), "s"(b.q) : "memory");
  else asm volatile("buffer_load_dword %0, %1, %2, 0 offen sc1" : "=v"(r) : "v"(voff), "s"(b.q) : "memory");
  return r;
}
template <int AUX> DI u32x4 ld16(const Buf& rs, int voff, int soff = 0) { return __builtin_amdgcn_raw_buffer_load_b128(rs.r, voff, soff, AUX); }
template <int AUX> DI u32x2 ld8(const Buf& rs, int voff, int soff = 0) { return __builtin_amdgcn_raw_buffer_load_b64(rs.r, voff, soff, AUX); }
template <int AUX> DI void st16(const Buf& rs, u32x4 v, int voff) { __builtin_amdgcn_raw_buffer_store_b128(v, rs.r, voff, 0, AUX); }
template <int AUX> DI void st8(const Buf& rs, u32x2 v, int voff, int soff = 0) { __builtin_amdgcn_raw_buffer_store_b64(v, rs.r, voff, soff, AUX); }
DI bf16x8 as_frag(u32x4 v) { return __builtin_bit_cast(bf16x8, v); }
DI bf16x8 lds_frag(const char* p) { return *(const LAS bf16x8*)(p); }
DI bf16x4 lds_tr(const char* p) { return __builtin_amdgcn_ds_read_tr16_b64_v4bf16((LAS bf16x4*)(p)); }
DI bf16x8 cat(bf16x4 a, bf16x4 b) { return __builtin_shufflevector(a, b, 0, 1, 2, 3, 4, 5, 6, 7); }
DI unsigned pk2(float lo, float hi) { f32x2 v = {lo, hi}; return __builtin_bit_cast(unsigned, __builtin_convertvector(v, bf16x2)); }
DI float rbf(float v) { return __uint_as_float(pk2(v, v) << 16); }
DI float bflo(unsigned u) { return __uint_as_float(u << 16); }
DI float bfhi(unsigned u) { return __uint_as_float(u & 0xffff0000u); }
DI float dot2(unsigned a, unsigned b, float c) {
  return __builtin_amdgcn_fdot2_f32_bf16(__builtin_bit_cast(bf16x2, a), __builtin_bit_cast(bf16x2, b), c, false);
}
DI float sfma(float a, float b, float c) { float d; asm("v_fma_f32 %0, %1, %2, %3" : "=v"(d) : "v"(a), "v"(b), "v"(c)); return d; }
DI float sadd(float a, float b) { float d; asm("v_add_f32 %0, %1, %2" : "=v"(d) : "v"(a), "v"(b)); return d; }
DI float smul(float a, float b) { float d; asm("v_mul_f32 %0, %1, %2" : "=v"(d) : "v"(a), "v"(b)); return d; }
DI float sfma_t(float a, float b, float c) { float d; asm("s_nop 0\n\tv_fma_f32 %0, %1, %2, %3" : "=v"(d) : "v"(a), "v"(b), "v"(c)); return d; }
DI float smul_t(float a, float b) { float d; asm("s_nop 0\n\tv_mul_f32 %0, %1, %2" : "=v"(d) : "v"(a), "v"(b)); return d; }
#define MFMA32(a, b, c) __builtin_amdgcn_mfma_f32_32x32x16_bf16((a), (b), (c), 0, 0, 0)
#define MFMA16(a, b, c) __builtin_amdgcn_mfma_f32_16x16x32_bf16((a), (b), (c), 0, 0, 0)
#define WAITV(n) asm volatile("s_waitcnt vmcnt(" #n ")" ::: "memory")
#define WAITVN(n) asm volatile("s_waitcnt vmcnt(%0)" :: "n"(n) : "memory")
#define WAITL() asm volatile("s_waitcnt lgkmcnt(0)" ::: "memory")
#define SB() __builtin_amdgcn_sched_barrier(0)
#define BAR() do { asm volatile("s_waitcnt lgkmcnt(0)\n\ts_barrier" ::: "memory"); } while (0)
constexpr unsigned ONES2 = 0x3f803f80u;

DI float xhalf(float v) { return __shfl_xor(v, 32); }
DI void set_flag(void* ws, int off, unsigned tag) {
  __hip_atomic_store((GAS unsigned*)((GAS char*)ws + off), tag, __ATOMIC_RELAXED, __HIP_MEMORY_SCOPE_AGENT);
}
DI void set_line(const Buf& rs, int off, unsigned tag, int lane) { __builtin_amdgcn_raw_buffer_store_b32(tag, rs.r, lane < 32 ? off + lane * 4 : OOB, 0, SC1); }
DI void wait_flags(void* ws, int off, unsigned tag) {
  GAS unsigned* p = (GAS unsigned*)((GAS char*)ws + off);
#pragma unroll 1
  for (int spin = 0; spin < (1 << 17); ++spin) {
    unsigned v = __hip_atomic_load(p, __ATOMIC_RELAXED, __HIP_MEMORY_SCOPE_AGENT);
    if (__all(v == tag)) break;
    __builtin_amdgcn_s_sleep(1);
  }
  __builtin_amdgcn_fence(__ATOMIC_ACQUIRE, "wavefront");
}
template <int CTRL> DI float dpp_add(float v) { return v + __builtin_bit_cast(float, __builtin_amdgcn_update_dpp(0, __builtin_bit_cast(int, v), CTRL, 0xf, 0xf, true)); }
DI float wave_sum(float v) {
  v = dpp_add<0xB1>(v); v = dpp_add<0x4E>(v); v = dpp_add<0x141>(v); v = dpp_add<0x140>(v);
  v += __shfl_xor(v, 16); v += __shfl_xor(v, 32);
  return v;
}
DI void ln_stats(float sum, float sq, float& mean, float& rstd) {
  float m = sum * (1.0f / 640.0f);
  float var = fmaxf(sq * (1.0f / 640.0f) - m * m, 0.0f);
  mean = rbf(m);
  float s = rbf(__builtin_amdgcn_sqrtf(rbf(rbf(var) + 1e-5f)));
  rstd = __builtin_amdgcn_rcpf(s);
}

#define STAMP(i) do {} while (0)
#define STAMPC(i) do {} while (0)
#define STAMP_DECL do {} while (0)
#define STAMP_FLUSH() do {} while (0)

constexpr int CHS = 6144 + 64;
template <bool EDGE, class F, class G, class H>
DI void attn_body(char* lds, int KB, int VB, int ONE, int hh, int qt, int i0, int lane, bf16x8 q0, bf16x8 q1, f32x16& O, F vwait, G mid, H side) {
  const int l31 = lane & 31, h = lane >> 5;
  const char* kp0 = lds + KB + (32 * qt + l31) * 16 + ((hh ? 3 : 0) + h) * CHS;
  const char* kp1 = lds + KB + (32 * qt + l31) * 16 + 2 * CHS;
  constexpr float NEG = -1e30f;
  f32x16 S[8];
  bf16x8 k0[9], k1[9];
  constexpr int ord[9] = {8, 0, 1, 2, 3, 4, 5, 6, 7};
#pragma unroll
  for (int n = 0; n < 3; ++n) { k0[n] = lds_frag(kp0 + ord[n] * 512); k1[n] = lds_frag(kp1 + ord[n] * 512); }
  float m0 = NEG, m1 = NEG;
  f32x16 D;
#pragma unroll
  for (int n = 0; n < 9; ++n) {
    if (n + 3 < 9) { k0[n + 3] = lds_frag(kp0 + ord[n + 3] * 512); k1[n + 3] = lds_frag(kp1 + ord[n + 3] * 512); }
    f32x16 z = {};
    z = MFMA32(k0[n], q0, z);
    z = MFMA32(k1[n], q1, z);
    if (n == 0) D = z; else S[n - 1] = z;
    if (n >= 2) {
      const int i = n - 2;
      if (EDGE) {
#pragma unroll
        for (int r = 0; r < 16; ++r) S[i][r] = (i < i0) ? NEG : S[i][r];
      }
      if (i == 0) {
#pragma unroll
        for (int r = 0; r < 16; ++r) S[0][r] = ((r & 3) + 8 * (r >> 2) + 4 * h <= l31) ? D[r] : S[0][r];
      }
#pragma unroll
      for (int r = 0; r < 16; r += 4) { m0 = fmaxf(fmaxf(m0, S[i][r]), S[i][r + 1]); m1 = fmaxf(fmaxf(m1, S[i][r + 2]), S[i][r + 3]); }
      asm volatile("" : "+v"(m0), "+v"(m1));
    }
    SB();
    if (n < 8) side(n);
  }
  if (EDGE) {
#pragma unroll
    for (int r = 0; r < 16; ++r) S[7][r] = (7 < i0) ? NEG : S[7][r];
  }
#pragma unroll
  for (int r = 0; r < 16; r += 4) { m0 = fmaxf(fmaxf(m0, S[7][r]), S[7][r + 1]); m1 = fmaxf(fmaxf(m1, S[7][r + 2]), S[7][r + 3]); }
  float m = fmaxf(m0, m1);
  m = fmaxf(m, xhalf(m));
  constexpr float C = 0.22360679774997896f * 1.4426950408889634f;
  const f32x2 C2 = {C, C}, mc2 = {-m * C, -m * C};
  const int gi = lane >> 4, dh = gi & 1, q = (lane & 15) >> 2, p = lane & 3;
  const bool one = dh && (p == 1 || p == 2);
  const int c4 = min(hh * 5 + dh * 4 + p, 9);
  const int vbase = one ? ONE : (VB + (32 * qt + 4 * h + q) * 16 + (c4 >> 1) * CHS + (c4 & 1) * 8);
  const int vstr = one ? 0 : 128;
  vwait();
  auto vfrag = [&](int i, int s) { return cat(lds_tr(lds + vbase + vstr * (4 * i + 2 * s)), lds_tr(lds + vbase + vstr * (4 * i + 2 * s + 1))); };
  O = f32x16{};
  bf16x8 vf[2][2];
  vf[1][0] = vfrag(1, 0); vf[1][1] = vfrag(1, 1);
  auto scale = [&](int i) {
#pragma unroll
    for (int r = 0; r < 16; r += 2) {
      f32x2 a = {S[i][r], S[i][r + 1]};
      a = __builtin_elementwise_fma(a, C2, mc2);
      S[i][r] = a.x; S[i][r + 1] = a.y;
    }
    asm volatile("" : "+v"(S[i]));
  };
  scale(1);
  SB();
#pragma unroll
  for (int n = 1; n <= 8; ++n) {
    const int i = n & 7;
    if (n == 2) mid();
    if (n < 8) { vf[(n + 1) & 1][0] = vfrag((n + 1) & 7, 0); vf[(n + 1) & 1][1] = vfrag((n + 1) & 7, 1); }
    else { vf[1][0] = vfrag(8, 0); vf[1][1] = vfrag(8, 1); }
#pragma unroll
    for (int r = 0; r < 16; ++r) S[i][r] = __builtin_amdgcn_exp2f(S[i][r]);
    asm volatile("" : "+v"(S[i]));
    SB();
    u32x4 pk[2];
#pragma unroll
    for (int j = 0; j < 8; ++j) pk[j >> 2][j & 3] = pk2(S[i][2 * j], S[i][2 * j + 1]);
    if (n < 8) scale((n + 1) & 7);
    asm volatile("" : "+v"(pk[0]), "+v"(pk[1]));
    SB();
#pragma unroll
    for (int s = 0; s < 2; ++s) {
      if (n < 8) O = MFMA32(vf[n & 1][s], as_frag(pk[s]), O);
      else {
        u32x4 pd, po;
#pragma unroll
        for (int j = 0; j < 4; ++j) {
          const int r = 8 * s + 2 * j, kk = (r & 3) + 8 * (r >> 2) + 4 * h;
          const unsigned mk = (kk <= l31 ? 0xffffu : 0u) | (kk + 1 <= l31 ? 0xffff0000u : 0u);
          pd[j] = pk[s][j] & mk; po[j] = pk[s][j] & ~mk;
        }
        O = MFMA32(vf[0][s], as_frag(po), O);
        O = MFMA32(vf[1][s], as_frag(pd), O);
      }
    }
    SB();
  }
}

#define SRC_T 7
constexpr int p1_wait(int t) {
  int n = 0;
  if (t == 0) return 4;
  if (t == 1) n += 1;
  for (int k = t + 1; k <= t + 2 && k < 10; ++k) n += 4;
  const int it_of_tile = t < 2 ? -1 : (t == 2 ? -1 : t - 3);
  if (6 > it_of_tile && 6 < t) n += 2;
  if (SRC_T > it_of_tile && SRC_T < t) n += 4;
  return n;
}
__global__ void __launch_bounds__(512, 2) fused_block(Params P) {
  extern __shared__ __attribute__((aligned(16))) char lds[];
  const int tid = threadIdx.x, lane = tid & 63;
  const int w = __builtin_amdgcn_readfirstlane(tid >> 6);
  const int bid = blockIdx.x, xcd = bid & 7, slot = bid >> 3;
  const int l31 = lane & 31, h = lane >> 5, l15 = lane & 15, g = lane >> 4;
  const int q4 = l15 >> 2, p4 = lane & 3, r8 = lane >> 3, c8 = (lane & 7) * 16;
  const Buf rws = mk_rsrc(P.ws, WS_BYTES);
  const Buf rx = mk_rsrc(P.x, 2048 * 640 * 2);
  u64 pc0; asm volatile("s_getpc_b64 %0" : "=s"(pc0));
  STAMP_DECL;
  STAMP(0);
  unsigned epoch;
  unsigned tag;
  const int panel1 = 4 * (xcd >> 1) + (slot & 3), ct = 8 * (xcd & 1) + (slot >> 2);
  u32x4 pkf[4] = {}; float pkc[2] = {0.f, 0.f};
  int pk_dst[4] = {OOB, OOB, OOB, OOB}, pk_cd = OOB, pk_flag = OOB;
  bool wo_ok = false;
  const int srow = panel1 * 128 + ct * 8 + w;
  u32x4 sx4; unsigned sx1;
  auto auxload = [&]() {
    epoch = hld4<SC1>(rws, lane == 0 ? OFF_EPOCH : OOB);
    sx4 = hld16<0>(rx, srow * 1280 + lane * 20);
    sx1 = hld4<0>(rx, srow * 1280 + lane * 20 + 16);
    const Buf rc = mk_rsrc((const void*)(pc0 & ~127ull), CODE_SPAN);
    dma4(rc, lds + 163072, ((slot * 8 + w) * 256 + lane * 128) | (lane < 2 ? 0 : OOB), 0);
  };
  auto xstats = [&]() {
    float v[10] = {bflo(sx4[0]), bfhi(sx4[0]), bflo(sx4[1]), bfhi(sx4[1]), bflo(sx4[2]), bfhi(sx4[2]), bflo(sx4[3]), bfhi(sx4[3]), bflo(sx1), bfhi(sx1)};
    float sum = 0.f;
#pragma unroll
    for (int i = 0; i < 10; ++i) sum += v[i];
    const float m = wave_sum(sum) * (1.0f / 640.0f);
    float sq = 0.f;
#pragma unroll
    for (int i = 0; i < 10; ++i) sq += (v[i] - m) * (v[i] - m);
    const float var = wave_sum(sq) * (1.0f / 640.0f);
    const float sd = __builtin_amdgcn_sqrtf(rbf(rbf(var) + 1e-5f));
    tag = __builtin_amdgcn_readfirstlane(epoch * 1664525u + 1013904223u);
    st8<SC1>(rws, u32x2{pk2(m, sd), tag}, lane == 0 ? OFF_XST + srow * 8 : OOB);
  };

  constexpr int KB = 0, VB = 5 * CHS, WOB = 10 * CHS, MISC = 161792;
  static_assert(WOB + 95 * 1024 <= MISC, "LDS map");
  const int cg3 = slot >> 2;
  auto wopiece = [&](int i) {
    const int pc = min((w & 3) + 4 * i, 94), nt = pc / 19, s = pc - nt * 19;
    dma16<SC1>(rws, lds + WOB + pc * 1024, lane * 16, OFF_WO + ((5 * cg3 + nt) * 20 + s) * 1024);
  };

  if (__builtin_expect(ct < 15, 1)) {
    const int panel = panel1;
    const Buf rw = mk_rsrc(P.wqkv, 640 * 1920 * 2);
    constexpr int XS = 0, WSB = 65536, ST = 131072, CSP = ST + 2048, CSF = CSP + 2048, TCH = CSF + 1024;
    const int xv0 = (panel * 128 + 16 * w + r8) * 1280 + ((lane & 7) ^ (r8 >> 1)) * 16;
    const int xv1 = (panel * 128 + 16 * w + 8 + r8) * 1280 + ((lane & 7) ^ (4 + (r8 >> 1))) * 16;
    const int wv = (8 * w + g) * 3840 + ct * 256 + (((q4 ^ g) << 2) | p4) * 16;
#define ROTX ((3 * (slot & 3) + (slot >> 2) + 5 * (xcd >> 2)) % 10)
    const int rot = ROTX;
    auto issue1 = [&](int t, int i) {
      char* xs = lds + XS + (t & 3) * 16384 + w * 2048;
      char* ws_ = lds + WSB + (t & 3) * 16384 + w * 2048;
      const int kt = t + rot >= 10 ? t + rot - 10 : t + rot;
      if (i == 0) dma16<0>(rx, xs, xv0, kt * 128);
      if (i == 1) dma16<0>(rw, ws_, wv, kt * 245760);
      if (i == 2) dma16<0>(rx, xs + 1024, xv1, kt * 128);
      if (i == 3) dma16<0>(rw, ws_ + 1024, wv, kt * 245760 + 15360);
    };
    auto issue = [&](int t) { issue1(t, 0); issue1(t, 1); issue1(t, 2); issue1(t, 3); };
    constexpr int AH = 3;
    STAMP(29);
    issue(0); STAMP(30); __builtin_amdgcn_s_barrier();
    auxload(); __builtin_amdgcn_s_barrier();
    issue(1); STAMP(31);
    STAMP(23);
    const int tt = w & 3, nh = w >> 2, f = tt ^ (2 * nh);
    const int row = 32 * tt + l31;
    const int e = (2 * f) ^ h ^ ((row >> 1) & 7);
    const char* xa[4]; const char* wa[4][2];
#pragma unroll
    for (int s = 0; s < 4; ++s) {
      xa[s] = lds + XS + row * 128 + (((2 * s) ^ e) * 16);
#pragma unroll
      for (int j = 0; j < 2; ++j)
        wa[s][j] = lds + WSB + (16 * (s ^ f) + 8 * h + q4) * 256 + (((2 * nh + j) ^ q4) * 64) + 32 * (g & 1) + 8 * p4;
    }
    f32x16 acc[2] = {};
    float cs[2] = {0.f, 0.f};
    bf16x8 xf[2][4], wf[2][4][2];
    u32x2 xs; u32x4 src[4]; unsigned fw;
    const int u = (bid < 224 ? bid : 112 + (bid >> 1)) * 5 + w;
    const bool dopk = w < 5 && u < 1080;
    const int pcb = u % 27, pks = u / 27;
#pragma unroll
    for (int t = 0; t <= 10; ++t) {
      if (t < 10) {
        WAITVN(p1_wait(t < 10 ? t : 9));
        BAR();
        if (t == 0) STAMP(24);
      }
      if (t == 0) { asm volatile("" : "+v"(epoch), "+v"(sx4), "+v"(sx1)); xstats(); issue(2); }
      if (t == 6) {
        xs = hld8<SC1>(rws, OFF_XST + (panel * 128 + row) * 8);
        fw = hld4<SC1>(rws, OFF_FW + l15 * 128);
      }
      if (t == SRC_T) {
        const Buf rg = mk_rsrc(P.wg, 640 * 1728 * 2), ru = mk_rsrc(P.wu, 640 * 1728 * 2);
        const int v = dopk ? (16 * pks + r8) * 3456 + pcb * 128 + c8 : OOB;
        src[0] = hld16<0>(rg, v); src[1] = hld16<0>(rg, v, 8 * 3456); src[2] = hld16<0>(ru, v); src[3] = hld16<0>(ru, v, 8 * 3456);
      }
      SB();
      const int so = (t & 3) * 16384, b = t & 1;
#pragma unroll
      for (int s = 0; s < 4; ++s) {
        if (t > 0) {
          acc[0] = MFMA32(wf[b ^ 1][s][0], xf[b ^ 1][s], acc[0]);
          acc[1] = MFMA32(wf[b ^ 1][s][1], xf[b ^ 1][s], acc[1]);
        }
        if (t < 10) {
          xf[b][s] = lds_frag(xa[s] + so);
          wf[b][s][0] = cat(lds_tr(wa[s][0] + so), lds_tr(wa[s][0] + so + 1024));
          wf[b][s][1] = cat(lds_tr(wa[s][1] + so), lds_tr(wa[s][1] + so + 1024));
        }
        if (t > 0) {
          const unsigned a = __builtin_bit_cast(u32x4, wf[b ^ 1][3][0])[s], c = __builtin_bit_cast(u32x4, wf[b ^ 1][3][1])[s];
          cs[0] = sadd(sadd(cs[0], bflo(a)), bfhi(a)); cs[1] = sadd(sadd(cs[1], bflo(c)), bfhi(c));
          asm volatile("" : "+v"(cs[0]), "+v"(cs[1]));
        }
        SB();
        if (t + AH < 10) issue1(t + AH, s);
        SB();
      }
    }
    STAMP(1);
    cs[0] += xhalf(cs[0]); cs[1] += xhalf(cs[1]);
    if (lane < 32) {
      *(LAS float*)(lds + CSP + (tt * 128 + 64 * nh + l31) * 4) = cs[0];
      *(LAS float*)(lds + CSP + (tt * 128 + 64 * nh + 32 + l31) * 4) = cs[1];
    }
    BAR();
    if (tid < 128) {
      const LAS float* c = (const LAS float*)(lds + CSP) + tid;
      *(LAS float*)(lds + CSF + tid * 4) = (c[0] + c[128]) + (c[256] + c[384]);
    }
    asm volatile("" : "+v"(xs), "+v"(fw));
#pragma unroll 1
    for (int spin = 0; spin < (1 << 17) && !__all(xs[1] == tag); ++spin) {
      __builtin_amdgcn_s_sleep(1);
      asm volatile("buffer_load_dwordx2 %0, %1, %2, 0 offen sc1\n\ts_waitcnt vmcnt(0)" : "=&v"(xs) : "v"(OFF_XST + (panel * 128 + row) * 8), "s"(rws.q) : "memory");
    }
    const float mean = bflo(xs[0]), rstd = __builtin_amdgcn_rcpf(bfhi(xs[0]));
    BAR();
    {
      f32x4 c[8];
#pragma unroll
      for (int i = 0; i < 8; ++i) c[i] = *(const LAS f32x4*)(lds + CSF + (64 * nh + 8 * i + 4 * h) * 4);
      SB();
      STAMP(22);
      const int qdst = OFF_QKV + ((ct * 16 + 8 * nh + h) * 2048 + panel * 128 + row) * 16;
#pragma unroll
      for (int i = 0; i < 8; i += 2) {
        unsigned pk[2][2];
#pragma unroll
        for (int e2 = 0; e2 < 2; ++e2) {
          const int j = (i + e2) >> 2, gq = (i + e2) & 3;
          const f32x4 cc = c[i + e2];
          pk[e2][0] = pk2((acc[j][4 * gq + 0] - mean * cc[0]) * rstd, (acc[j][4 * gq + 1] - mean * cc[1]) * rstd);
          pk[e2][1] = pk2((acc[j][4 * gq + 2] - mean * cc[2]) * rstd, (acc[j][4 * gq + 3] - mean * cc[3]) * rstd);
        }
        auto r0 = __builtin_amdgcn_permlane32_swap(pk[0][0], pk[1][0], false, false);
        auto r1 = __builtin_amdgcn_permlane32_swap(pk[0][1], pk[1][1], false, false);
        st16<SC1>(rws, u32x4{r0[0], r1[0], r0[1], r1[1]}, qdst + i * 2048 * 16);
      }
    }
    SB();
    wo_ok = __all(fw == tag);
    {
      char* sc = lds + w * 4096;
      asm volatile("s_waitcnt vmcnt(4)" : "+v"(src[0]), "+v"(src[1]), "+v"(src[2]), "+v"(src[3]) :: "memory");
#pragma unroll
      for (int i = 0; i < 4; ++i) *(LAS u32x4*)(sc + i * 1024 + lane * 16) = src[i];
      WAITL();
      const char* a = sc + (g & 1) * 2048 + (8 * h + q4) * 128 + 8 * p4;
#pragma unroll
      for (int i = 0; i < 4; ++i) pkf[i] = __builtin_bit_cast(u32x4, cat(lds_tr(a + 32 * i), lds_tr(a + 32 * i + 512)));
#pragma unroll
      for (int i = 0; i < 4; i += 2) {
        float a0 = 0.f, a1 = 0.f;
#pragma unroll
        for (int j = 0; j < 4; ++j) { a0 = dot2(pkf[i][j], ONES2, a0); a1 = dot2(pkf[i + 1][j], ONES2, a1); }
        auto r = __builtin_amdgcn_permlane32_swap(__float_as_uint(a0), __float_as_uint(a1), false, false);
        pkc[i >> 1] = __uint_as_float(r[0]) + __uint_as_float(r[1]);
      }
      pk_cd = dopk ? OFF_CSP + ((4 * pcb + h) * 40 + pks) * 128 + l31 * 4 : OOB;
#pragma unroll
      for (int i = 0; i < 4; ++i) pk_dst[i] = dopk ? OFF_WGU + ((4 * pcb + i) * 40 + pks) * 1024 + lane * 16 : OOB;
      pk_flag = dopk ? OFF_FG + (pcb * 40 + pks) * 128 : OOB;
    }
    WAITV(0);
    BAR();
    if (w == 0) set_line(rws, OFF_F1 + (panel * 16 + ct) * 128, tag, lane);
    STAMP(2);
    if (w >= 5) {
      const int ud = (bid < 224 ? bid : 112 + (bid >> 1)) * 3 + w - 5, cb = ud % 10, s = ud / 10;
      const Buf rd_ = mk_rsrc(P.wd, 1728 * 640 * 2);
      const int v = ud < 540 ? (8 * (r8 >> 2) + (r8 & 3)) * 1280 + c8 : OOB, b0 = 32 * s * 1280 + cb * 128;
      char* d = lds + w * 4096;
      dma16<0>(rd_, d, v, b0); dma16<0>(rd_, d + 1024, v, b0 + 4 * 1280); dma16<0>(rd_, d + 2048, v, b0 + 16 * 1280); dma16<0>(rd_, d + 3072, v, b0 + 20 * 1280);
      WAITV(0);
      const char* a = d + (8 * g + q4) * 128 + 8 * p4;
#pragma unroll
      for (int i = 0; i < 4; ++i) {
        pkf[i] = __builtin_bit_cast(u32x4, cat(lds_tr(a + 32 * i), lds_tr(a + 32 * i + 512)));
        const int nt = 4 * cb + i, m = nt / 5, j = nt - 5 * m;
        const int dst = j == 2 ? OFF_WD + ((2 * m + (l15 >> 3)) * 54 + s) * 2560 + 2048 + g * 128 + (l15 & 7) * 16
                               : OFF_WD + ((2 * m + (j > 2)) * 54 + s) * 2560 + (j > 2 ? j - 3 : j) * 1024 + lane * 16;
        pk_dst[i] = ud < 540 ? dst : OOB;
      }
      pk_flag = ud < 540 ? OFF_FD + (cb * 54 + s) * 128 : OOB;
    }
  } else {
    const Buf ro_ = mk_rsrc(P.wo, 640 * 640 * 2);
    const int rank = ((xcd >> 1) * 4 + (slot & 3)) * 8 + w;
    char* sc = lds + w * 8192;
    auxload();
    __builtin_amdgcn_s_barrier();
#pragma unroll
    for (int k = 0; k < 2; ++k) {
      const int u = rank + 128 * k, cb = u % 10, s = u / 10;
      char* d = sc + k * 4096;
      const int vo = u < 200 ? ((s < 16 ? 8 : 20) * (r8 >> 2) + (r8 & 3)) * 1280 + c8 : OOB;
      const int b0 = (s < 16 ? 40 * s : 160 * (s - 16) + 16) * 1280 + cb * 128, d1 = (s < 16 ? 4 : 40) * 1280, d2 = (s < 16 ? 20 : 80) * 1280;
      dma16<0>(ro_, d, vo, b0); dma16<0>(ro_, d + 1024, vo, b0 + d1); dma16<0>(ro_, d + 2048, vo, b0 + d2); dma16<0>(ro_, d + 3072, vo, b0 + d1 + d2);
    }
    asm volatile("s_waitcnt vmcnt(8)" : "+v"(epoch), "+v"(sx4), "+v"(sx1) :: "memory");
    xstats();
    STAMP(3);
    WAITV(1);
#pragma unroll
    for (int k = 0; k < 2; ++k) {
      const int u = rank + 128 * k, cb = u % 10, s = u / 10;
      const char* a = sc + k * 4096 + (8 * g + q4) * 128 + 8 * p4;
#pragma unroll
      for (int i = 0; i < 4; ++i) {
        bf16x8 fr = cat(lds_tr(a + 32 * i), lds_tr(a + 32 * i + 512));
        st16<SC1>(rws, __builtin_bit_cast(u32x4, fr), u < 200 ? OFF_WO + ((4 * cb + i) * 20 + s) * 1024 + lane * 16 : OOB);
      }
    }
    WAITV(0);
    BAR();
    if (w == 0) set_line(rws, OFF_FW + panel1 * 128, tag, lane);
    {
      const Buf rd_ = mk_rsrc(P.wd, 1728 * 640 * 2);
#pragma unroll 1
      for (int i = 0; i < 17; ++i) dma16<0>(rd_, sc, lane * 16 + (i * 128 + rank) * 1024, 0);
      WAITV(0);
      BAR();
    }
  }

  {
    const int panel = panel1, hp = ct;
    if (w == 2 && !wo_ok) wait_flags(P.ws, OFF_FW + l15 * 128, tag);
    if (w == 1) {
      const int ctA = (40 * hp) >> 7, ctB = (40 * hp + 39) >> 7;
      const int i = lane < 14 ? lane : 0;
      const int sec = i < 2 ? 0 : (i < 8 ? 1 : 2);
      const int rem = i < 2 ? i : (i < 8 ? i - 2 : i - 8);
      int pp = panel - (rem >> 1); pp = pp < 0 ? panel : pp;
      wait_flags(P.ws, OFF_F1 + (pp * 16 + sec * 5 + ((rem & 1) ? ctB : ctA)) * 128, tag);
    }
    if (tid == 0) { *(LAS u32x4*)(lds + MISC) = u32x4{0x00003f80u, 0u, 0u, 0u}; *(LAS u32x4*)(lds + MISC + 16) = u32x4{}; *(LAS u32x4*)(lds + MISC + 32) = u32x4{}; *(LAS u32x4*)(lds + MISC + 2016) = u32x4{}; *(LAS u32x4*)(lds + MISC + 2032) = u32x4{}; }
    BAR();
    STAMP(4);
    const int hh = w & 1, qt = w >> 1;
    const int tok0 = 128 * (panel - 2);
    const int qoff = OFF_QKV + (5 * hp * 2048 + 128 * panel + 32 * qt + l31) * 16;
    u32x4 q0 = hld16<SC1>(rws, qoff + ((hh ? 3 : 0) + h) * 32768);
    u32x4 q1 = hld16<SC1>(rws, h ? OOB : qoff + 2 * 32768);
#pragma unroll
    for (int i = 0; i < 8; ++i) {
      const int isv = i >> 2, pi = min(w + 8 * (i & 3), 29), ch = pi / 6, part = pi - 6 * ch;
      const int rel = tok0 + part * 64;
      dma16<SC1>(rws, lds + isv * VB + ch * CHS + part * 1024, rel < 0 ? OOB : lane * 16, OFF_QKV + (((1 + isv) * 80 + 5 * hp + ch) * 2048 + (rel < 0 ? 0 : rel)) * 16);
      if (i == 3) __builtin_amdgcn_s_barrier();
    }
    asm volatile("s_waitcnt vmcnt(4)" : "+v"(q0), "+v"(q1) :: "memory");
    if (hh) { q1[0] = 0; q1[1] = 0; } else { q1[2] = 0; q1[3] = 0; }
    BAR();
    STAMP(5);
    auto vflag = [&]() { if (lane == 0) *(volatile LAS unsigned*)(lds + MISC + 2016 + w * 4) = 1u; };
    if (w < 4) __builtin_amdgcn_s_setprio(2);
    else { WAITV(0); vflag(); }
#pragma unroll
    for (int i = 0; i < 4; ++i) st16<SC1>(rws, pkf[i], pk_dst[i]);
    __builtin_amdgcn_raw_buffer_store_b32(__float_as_uint(pkc[0]), rws.r, pk_cd, 0, SC1);
    __builtin_amdgcn_raw_buffer_store_b32(__float_as_uint(pkc[1]), rws.r, pk_cd + 2 * 40 * 128, 0, SC1);
    STAMP(25);
    auto side = [&](int n) { if (w >= 4) { wopiece(3 * n); wopiece(3 * n + 1); wopiece(3 * n + 2); } };
    auto vwait = [&]() {
      STAMP(26);
      if (w < 4) { WAITV(6); vflag(); }
#pragma unroll 1
      for (int spin = 0; spin < (1 << 22); ++spin) {
        if (__all(*(volatile LAS unsigned*)(lds + MISC + 2016 + (lane & 7) * 4) != 0u)) break;
        __builtin_amdgcn_s_sleep(1);
      }
      asm volatile("" ::: "memory");
      STAMP(27);
    };
    auto mid = [&]() {
      WAITV(0);
      set_line(rws, pk_flag, tag, lane);
      if (w >= 4 && lane == 0) *(volatile LAS unsigned*)(lds + MISC + 32 + (w & 3) * 4) = 1u;
      STAMP(28);
    };
    f32x16 O;
    if (panel >= 2) attn_body<false>(lds, KB, VB, MISC, hh, qt, 0, lane, as_frag(q0), as_frag(q1), O, vwait, mid, side);
    else attn_body<true>(lds, KB, VB, MISC, hh, qt, 8 - 4 * panel - qt, lane, as_frag(q0), as_frag(q1), O, vwait, mid, side);
    const float inv = __builtin_amdgcn_rcpf(h ? O[8] : O[12]);
    const int t32 = 4 * panel + qt;
    u32x4 ov = {pk2(O[0] * inv, O[1] * inv), pk2(O[2] * inv, O[3] * inv), pk2(O[4] * inv, O[5] * inv), pk2(O[6] * inv, O[7] * inv)};
    st16<SC1>(rws, ov, OFF_OF + ((t32 * 40 + 2 * hp + hh) * 64 + lane) * 16);
    u32x2 ol = {pk2(O[8] * inv, O[9] * inv), pk2(O[10] * inv, O[11] * inv)};
    st8<SC1>(rws, ol, h ? OOB : OFF_OF + (t32 * 40 + 32) * 1024 + ((2 * hp + hh) * 32 + l31) * 8);
    STAMP(6);
    WAITV(0);
    set_line(rws, OFF_F2 + (t32 * 32 + 2 * hp + hh) * 128, tag, lane);
    STAMP(7);
  }

  const int panel4 = 8 * (xcd >> 2) + (slot & 7), fg = 4 * (xcd & 3) + (slot >> 3);
  const int ntile = fg < 12 ? 7 : 6, tile = (fg < 12 ? 7 * fg : 84 + 6 * (fg - 12)) + (w < ntile ? w : 0);
  const int w4v = w < ntile ? lane * 16 : OOB, w4s = OFF_WGU + tile * 40 * 1024;
#define W4PRE 8
  u32x4 cs5[5];
  u32x4 w4[40];
  {
    const int sub = w & 1, ng = (w >> 1) & 1;
    const int t32 = 8 * xcd + (slot & 1) + 4 * ((slot >> 1) & 1) + 2 * (w >> 2);
    const int token = 32 * t32 + 16 * sub + l15;
    u32x4 wl[3];
    u32x2 xr[3];
#pragma unroll
    for (int i = 0; i < 3; ++i) {
      wl[i] = ld16<SC1>(rws, (ng && i == 2) ? OOB : lane * 16, OFF_WO + ((5 * cg3 + 3 * ng + i) * 20 + 19) * 1024);
      xr[i] = ld8<0>(rx, (ng && i == 2) ? OOB : token * 1280 + (80 * cg3 + 16 * (3 * ng + i) + 4 * g) * 2);
    }
    if (w < ntile) wait_flags(P.ws, OFF_FG + ((tile >> 2) * 40 + min(lane, 39)) * 128, tag);
#pragma unroll
    for (int k = 0; k < W4PRE; ++k) w4[k] = ld16<SC1>(rws, w4v, w4s + k * 1024);
#pragma unroll
    for (int i = 0; i < 5; ++i) cs5[i] = ld16<SC1>(rws, w4v, OFF_CSP + tile * 5120 + i * 1024);
    SB();
    const char* wb = lds + WOB + ng * 3 * 19 * 1024 + lane * 16;
    const int t2 = ng ? 19 * 1024 : 2 * 19 * 1024;
    bf16x8 wf[4][3];
    auto wread = [&](int s) { wf[s & 3][0] = lds_frag(wb + s * 1024); wf[s & 3][1] = lds_frag(wb + (19 + s) * 1024); wf[s & 3][2] = lds_frag(wb + t2 + s * 1024); };
#pragma unroll 1
    for (int spin = 0; spin < (1 << 22); ++spin) {
      if (__all(*(volatile LAS unsigned*)(lds + MISC + 32 + p4 * 4) != 0u)) break;
      __builtin_amdgcn_s_sleep(1);
    }
    asm volatile("" ::: "memory");
    wread(0); wread(1); wread(2);
    SB();
    wait_flags(P.ws, OFF_F2 + (t32 * 32 + l31) * 128, tag);
    STAMP(8);
    const int ov = OFF_OF + ((t32 * 40 + (g >> 1)) * 64 + (g & 1) * 32 + sub * 16 + l15) * 16;
    u32x4 of[20];
#pragma unroll
    for (int s = 0; s < 16; ++s) of[s] = ld16<SC1>(rws, ov, s * 2048);
    const int ol = OFF_OF + (t32 * 40 + 32) * 1024 + (2 * g * 32 + sub * 16 + l15) * 8;
#pragma unroll
    for (int s = 16; s < 20; ++s) {
      const u32x2 a = ld8<SC1>(rws, ol, (s - 16) * 2048), b = ld8<SC1>(rws, ol + 256, (s - 16) * 2048);
      of[s] = u32x4{a[0], a[1], b[0], b[1]};
    }
    SB();
    f32x4 acc[3] = {};
#pragma unroll
    for (int s = 0; s < 19; ++s) {
      if (s + 3 < 19) wread(s + 3);
      acc[0] = MFMA16(wf[s & 3][0], as_frag(of[s]), acc[0]);
      acc[1] = MFMA16(wf[s & 3][1], as_frag(of[s]), acc[1]);
      if (!ng) acc[2] = MFMA16(wf[s & 3][2], as_frag(of[s]), acc[2]);
      SB();
    }
#pragma unroll
    for (int i = 0; i < 3; ++i) acc[i] = MFMA16(as_frag(wl[i]), as_frag(of[19]), acc[i]);
    STAMP(15);
    float sum = 0.f, sq = 0.f;
#pragma unroll
    for (int i = 0; i < 3; ++i) {
      const unsigned a0 = pk2(acc[i][0], acc[i][1]), a1 = pk2(acc[i][2], acc[i][3]);
      const u32x2 hv = {pk2(bflo(xr[i][0]) + bflo(a0), bfhi(xr[i][0]) + bfhi(a0)), pk2(bflo(xr[i][1]) + bflo(a1), bfhi(xr[i][1]) + bfhi(a1))};
      const int kap = 5 * cg3 + 3 * ng + i;
      st8<SC1>(rws, hv, (ng && i == 2) ? OOB : OFF_HF + ((t32 * 40 + kap) * 64 + (g >> 1) * 32 + sub * 16 + l15) * 16 + (g & 1) * 8);
      sum = dot2(hv[0], ONES2, sum); sum = dot2(hv[1], ONES2, sum);
      sq = dot2(hv[0], hv[0], sq); sq = dot2(hv[1], hv[1], sq);
    }
    sum += __shfl_xor(sum, 16); sq += __shfl_xor(sq, 16);
    sum += xhalf(sum); sq += xhalf(sq);
    st8<SC1>(rws, u32x2{__float_as_uint(sum), __float_as_uint(sq)}, lane < 16 ? OFF_HST + ((t32 * 16 + cg3 * 2 + ng) * 32 + 16 * sub + l15) * 8 : OOB);
    STAMP(9);
    WAITV(0);
    set_line(rws, OFF_F3 + (t32 * 32 + cg3 * 4 + (w & 3)) * 128, tag, lane);
    __builtin_amdgcn_s_setprio(0);
    STAMP(10);
  }

  constexpr int WDA = 81920, STT = MISC + 64, CSX = MISC + 1088;
  const int panel5 = 2 * xcd + (slot & 1), cg5 = slot >> 1;
  auto wdpiece = [&](int part, int pc) {
    dma16<SC1>(rws, lds + (part ? 0 : WDA) + pc * 1024, (pc * 1024 + lane * 16 < (part ? 23 : 31) * 2560) ? lane * 16 + OFF_WD + (cg5 * 54 + (part ? 31 : 0)) * 2560 + pc * 1024 : OOB, 0);
  };
  {
    const int panel = panel4;
    const int hsrc = OFF_HF + 4 * panel * 40 * 1024;
    auto hload = [&](int t) {
#pragma unroll
      for (int i = 0; i < 5; ++i) {
        const int sl = w + 8 * i;
        dma16<SC1>(rws, lds + (t & 1) * 40960 + sl * 1024, lane * 16, hsrc + (t < 2 ? (sl & 1) * 40 + 20 * t + (sl >> 1) : t * 40 + sl) * 1024);
      }
    };
    auto wdload = [&](int t) {
      if (t == 1) {
#pragma unroll
        for (int i = 0; i < 10; ++i) wdpiece(0, (w - 4) * 10 + i);
      } else if (t == 2) {
#pragma unroll
        for (int i = 0; i < 5; ++i) wdpiece(0, min(40 + w * 5 + i, 77));
      } else {
#pragma unroll
        for (int i = 0; i < 5; ++i) wdpiece(1, w * 5 + i);
      }
    };
    if (w >= ntile) {
      auto stats = [&](int pr) {
        u32x2 v[16];
#pragma unroll
        for (int i = 0; i < 16; ++i) v[i] = ld8<SC1>(rws, OFF_HST + (((4 * panel + 2 * pr + h) * 16 + i) * 32 + l31) * 8);
        float sum = 0.f, sq = 0.f;
#pragma unroll
        for (int i = 0; i < 16; ++i) { sum += __uint_as_float(v[i][0]); sq += __uint_as_float(v[i][1]); }
        float mean, rstd; ln_stats(sum, sq, mean, rstd);
        *(LAS f32x2*)(lds + STT + (64 * pr + lane) * 8) = f32x2{mean, rstd};
      };
      if (w == 7) { wait_flags(P.ws, OFF_F3 + (4 * panel * 32 + lane) * 128, tag); STAMP(11); }
      BAR();
      hload(0);
      WAITV(0);
      BAR();
      hload(1);
      if (w == 7) {
        stats(0);
        wait_flags(P.ws, OFF_FD + (((40 * cg5) >> 6) * 54 + min(lane, 53)) * 128, tag);
        wait_flags(P.ws, OFF_FD + (((40 * cg5 + 39) >> 6) * 54 + min(lane, 53)) * 128, tag);
      }
      WAITV(0);
      BAR(); STAMP(17);
      if (w == 7) {
        wait_flags(P.ws, OFF_F3 + ((4 * panel + 2) * 32 + lane) * 128, tag);
        if (lane == 0) *(volatile LAS unsigned*)(lds + MISC + 16) = 1u;
        stats(1);
      }
      wdload(1);
      BAR(); STAMP(18);
      hload(3); wdload(2);
      WAITV(0);
      BAR(); STAMP(19);
      if (w == 7) { set_line(rws, OFF_F4 + (4 * panel * 16 + fg) * 128, tag, lane); set_line(rws, OFF_F4 + ((4 * panel + 1) * 16 + fg) * 128, tag, lane); }
      wdload(3);
      BAR();
      if (w == 7) set_line(rws, OFF_F4 + ((4 * panel + 2) * 16 + fg) * 128, tag, lane);
      WAITV(0);
      BAR();
      if (w == 7) set_line(rws, OFF_F4 + ((4 * panel + 3) * 16 + fg) * 128, tag, lane);
    } else {
      const bool late = w >= 4;
      f32x4 csr[4];
      {
        f32x4 c = __builtin_bit_cast(f32x4, cs5[0]);
#pragma unroll
        for (int i = 1; i < 5; ++i) c += __builtin_bit_cast(f32x4, cs5[i]);
#pragma unroll
        for (int j = 0; j < 4; ++j) { c[j] += __shfl_xor(c[j], 8); c[j] += __shfl_xor(c[j], 16); c[j] += xhalf(c[j]); }
        if (lane < 8) *(LAS f32x4*)(lds + CSX + w * 128 + lane * 16) = c;
        WAITL();
#pragma unroll
        for (int i = 0; i < 4; ++i) csr[i] = *(const LAS f32x4*)(lds + CSX + w * 128 + (8 * i + 4 * h) * 4);
      }
      BAR();
      hload(0);
      WAITV(0);
      f32x16 acc[2];
      auto epi = [&](int t, const f32x16& ac) {
        const f32x2 st = *(const LAS f32x2*)(lds + STT + (32 * t + l31) * 8);
        const float nmean = -st.x, rs = st.y;
        constexpr float C0 = -2.0f * 0.7978845608028654f * 1.4426950408889634f, C1 = C0 * 0.044715f;
        const float r2 = smul(rs, rs);
        const float a0 = smul(C0, rs), a1 = smul(smul(C1, rs), r2), ir2 = __builtin_amdgcn_rcpf(r2);
        float a[8];
#pragma unroll
        for (int r = 0; r < 8; ++r) {
          const float G = sfma(nmean, csr[r >> 2][r & 3], ac[r]), U = sfma(nmean, csr[2 + (r >> 2)][r & 3], ac[r + 8]);
          const float ex = __builtin_amdgcn_exp2f(smul(G, sfma(smul(G, G), a1, a0)));
          a[r] = smul_t(smul(G, U), __builtin_amdgcn_rcpf(sfma_t(ex, ir2, ir2)));
        }
        st16<SC1>(rws, u32x4{pk2(a[0], a[1]), pk2(a[2], a[3]), pk2(a[4], a[5]), pk2(a[6], a[7])},
                  OFF_ACT + (((4 * panel + t) * 108 + tile) * 64 + lane) * 16);
      };
      auto pass = [&](auto tc) {
        constexpr int t = decltype(tc)::value;
        const char* hb = lds + (t & 1) * 40960 + lane * 16;
        bf16x8 hf[8];
#pragma unroll
        for (int k = 0; k < 6; ++k) hf[k] = lds_frag(hb + k * 1024);
        if (t == 0) { acc[0] = f32x16{}; acc[1] = f32x16{}; }
        if (t >= 2) acc[t & 1] = f32x16{};
        SB();
#pragma unroll
        for (int k = 0; k < 40; ++k) {
          if (k + 6 < 40) hf[(k + 6) & 7] = lds_frag(hb + (k + 6) * 1024);
          if (t < 2) acc[k & 1] = MFMA32(as_frag(w4[20 * t + (k >> 1)]), hf[k & 7], acc[k & 1]);
          else acc[t & 1] = MFMA32(as_frag(w4[k]), hf[k & 7], acc[t & 1]);
          if (t < 2 && (k & 1) && 20 * t + (k >> 1) + W4PRE < 40) w4[20 * t + (k >> 1) + W4PRE] = ld16<SC1>(rws, lane * 16, w4s + (20 * t + (k >> 1) + W4PRE) * 1024);
          SB();
        }
        asm volatile("s_nop 7\n\ts_nop 7" : "+v"(acc[0]), "+v"(acc[1]));
      };
      BAR(); STAMP(16);
      hload(1);
      pass(IC<0>{});
      WAITV(20);
      BAR(); STAMP(17);
      if (late) wdload(1);
      pass(IC<1>{});
      if (!late) {
#pragma unroll 1
        for (int spin = 0; spin < (1 << 22) && *(volatile LAS unsigned*)(lds + MISC + 16) == 0u; ++spin) __builtin_amdgcn_s_sleep(1);
        asm volatile("" ::: "memory");
#pragma unroll
        for (int i = 0; i < 10; ++i) dma16<SC1>(rws, lds + (w * 10 + i) * 1024, lane * 16, hsrc + (80 + w * 10 + i) * 1024);
        epi(0, acc[0]); epi(1, acc[1]); WAITV(2);
      } else WAITV(0);
      BAR(); STAMP(18);
      if (late) { hload(3); epi(0, acc[0]); epi(1, acc[1]); }
      pass(IC<2>{});
      if (!late) { hload(3); wdload(2); epi(2, acc[0]); WAITV(1); } else { wdload(2); WAITV(5); }
      BAR(); STAMP(19);
      if (late) { wdload(3); epi(2, acc[0]); }
      pass(IC<3>{});
      if (!late) { wdload(3); epi(3, acc[1]); WAITV(1); } else WAITV(0);
      STAMP(20);
      BAR();
      if (late) {
        epi(3, acc[1]);
        WAITV(0);
        BAR();
      }
      STAMP(21);
    }
    STAMP(12);
  }

  {
    const int t32 = 4 * panel5 + (w >> 1), sub = w & 1;
#pragma unroll
    for (int i = 0; i < 3; ++i) wdpiece(1, min(40 + w + 8 * i, 57));
    u32x2 hr[3];
    wait_flags(P.ws, OFF_F4 + (t32 * 16 + l15) * 128, tag);
    STAMP(13);
    const int av = OFF_ACT + ((t32 * 108 + (g >> 1)) * 64 + (g & 1) * 32 + sub * 16 + l15) * 16;
#define P5_AHEAD 10
    constexpr int AH5 = P5_AHEAD;
    u32x4 af[54];
#pragma unroll
    for (int i = 0; i < AH5; ++i) af[i] = ld16<SC1>(rws, av, i * 2048);
    SB();
    if (w < 4) BAR();
    f32x4 acc[3] = {};
    const int hoff = 2048 + g * 128 + (l15 & 7) * 16;
    const int m = cg5 >> 1, odd = cg5 & 1;
    bf16x8 wf[4][3];
    auto wread = [&](int s) {
      const char* wb = lds + (s < 31 ? WDA + s * 2560 : (s - 31) * 2560);
      wf[s & 3][0] = lds_frag(wb + lane * 16); wf[s & 3][1] = lds_frag(wb + 1024 + lane * 16); wf[s & 3][2] = lds_frag(wb + hoff);
    };
    wread(0); wread(1); wread(2);
#pragma unroll
    for (int s = 0; s < 54; ++s) {
      if (s == 44) BAR();
      if (s + 3 < 54) wread(s + 3);
      acc[0] = MFMA16(wf[s & 3][0], as_frag(af[s]), acc[0]);
      acc[1] = MFMA16(wf[s & 3][1], as_frag(af[s]), acc[1]);
      acc[2] = MFMA16(wf[s & 3][2], as_frag(af[s]), acc[2]);
      if (s + AH5 < 54) af[s + AH5] = ld16<SC1>(rws, av, (s + AH5) * 2048);
      if (s + AH5 == 54) {
#pragma unroll
        for (int i = 0; i < 3; ++i) {
          const int c4 = i < 2 ? 4 * (5 * m + 3 * odd + i) + g : 4 * (5 * m + 2) + 2 * odd + g;
          hr[i] = ld8<SC1>(rws, (i == 2 && g >= 2) ? OOB : OFF_HF + ((t32 * 40 + (c4 >> 2)) * 64 + ((c4 >> 1) & 1) * 32 + sub * 16 + l15) * 16 + (c4 & 1) * 8);
        }
      }
      SB();
    }
    const Buf ro = mk_rsrc(P.out, 2048 * 640 * 2);
    const int token = 32 * t32 + 16 * sub + l15;
#pragma unroll
    for (int i = 0; i < 3; ++i) {
      const unsigned y0 = pk2(acc[i][0], acc[i][1]), y1 = pk2(acc[i][2], acc[i][3]);
      const u32x2 ov = {pk2(bflo(hr[i][0]) + bflo(y0), bfhi(hr[i][0]) + bfhi(y0)), pk2(bflo(hr[i][1]) + bflo(y1), bfhi(hr[i][1]) + bfhi(y1))};
      const int col = i < 2 ? 16 * (5 * m + 3 * odd + i) + 4 * g : 16 * (5 * m + 2) + 8 * odd + 4 * g;
      st8<0>(ro, ov, (i == 2 && g >= 2) ? OOB : token * 1280 + col * 2);
    }
    if (bid == 0 && w == 0) {
#pragma unroll 1
      for (int spin = 0; spin < (1 << 17); ++spin) {
        bool ok = true;
#pragma unroll
        for (int i = 0; i < 16; ++i) { const u32x4 v = ld16<SC1>(rws, OFF_XST + lane * 16, i * 1024); ok = ok && v[1] == tag && v[3] == tag; }
        asm volatile("" ::: "memory");
        if (__all(ok)) break;
        __builtin_amdgcn_s_sleep(1);
      }
      if (lane == 0) set_flag(P.ws, OFF_EPOCH, tag);
    }
    STAMP(14);
    STAMP_FLUSH();
  }
}

extern "C" void kernel_launch(void* const* d_in, const int* in_sizes, int n_in, void* d_out, int out_size, void* d_ws,
                              size_t ws_size, hipStream_t stream) {
  (void)in_sizes; (void)n_in; (void)out_size; (void)ws_size;
  Params p;
  p.x = d_in[0]; p.wqkv = d_in[1]; p.wo = d_in[2]; p.wg = d_in[4]; p.wu = d_in[5]; p.wd = d_in[6];
  p.out = d_out; p.ws = d_ws;
  (void)hipFuncSetAttribute((const void*)fused_block, hipFuncAttributeMaxDynamicSharedMemorySize, LDS_BYTES);
  fused_block<<<256, 512, LDS_BYTES, stream>>>(p);
}
```
